```python
import torch

def _softmax(x, axis=-1):
    return torch.softmax(x, dim=axis)

def _gelu(x):
    return torch.nn.functional.gelu(x, approximate='tanh')

def _silu(x):
    return torch.nn.functional.silu(x)

def _sigmoid(x):
    return torch.sigmoid(x)

def _layernorm(x, axis=-1, eps=1e-05):
    m = x.mean(dim=axis, keepdim=True)
    v = x.var(dim=axis, keepdim=True, unbiased=False)
    return (x - m) / torch.sqrt(v + eps)

def _scatter_add(tgt, idx, src, axis=0):
    return tgt.clone().index_add_(axis, idx, src)

def _segment_sum(src, seg, n):
    out = torch.zeros((n,) + src.shape[1:], dtype=src.dtype)
    return out.index_add_(0, seg, src)

def _pad(x, pw):
    flat = tuple((v for pair in reversed(pw) for v in pair))
    return torch.nn.functional.pad(x, flat)
kernel_name = 'standalone__dit_block__diffusion'

def setup_inputs():

    _w = lambda a: (lambda t: t.to(torch.bfloat16) if t.is_floating_point() else t)(torch.as_tensor(a))
    B, N, D = (2, 1024, 384)
    F = 4 * D
    return {'x': _w((lambda *s: torch.randn(*s, dtype=torch.bfloat16) * 0.05)(B, N, D)), 'c': _w((lambda *s: torch.randn(*s, dtype=torch.bfloat16) * 0.2)(B, D)), 'wqkv': _w((lambda *s: torch.randn(*s, dtype=torch.bfloat16) * 0.05)(D, 3 * D)), 'wo': _w((lambda *s: torch.randn(*s, dtype=torch.bfloat16) * 0.05)(D, D)), 'w_mod': _w((lambda *s: torch.randn(*s, dtype=torch.bfloat16) * 0.2)(D, 6 * D)), 'w_fc1': _w((lambda *s: torch.randn(*s, dtype=torch.bfloat16) * 0.05)(D, F)), 'w_fc2': _w((lambda *s: torch.randn(*s, dtype=torch.bfloat16) * 0.05)(F, D))}

def reference(x, c, wqkv, wo, w_mod, w_fc1, w_fc2):
    B, N, D = x.shape
    H = 2
    Dh = D // H
    mod = (_silu(c) @ w_mod).reshape(B, 6, 1, D)
    shift_a, scale_a, gate_a, shift_m, scale_m, gate_m = [mod[:, i] for i in range(6)]
    h = _layernorm(x, axis=-1) * (1 + scale_a) + shift_a
    qkv = (h @ wqkv).reshape(B, N, 3, H, Dh)
    q, k, v = (qkv[:, :, 0].clone(), qkv[:, :, 1].clone(), qkv[:, :, 2].clone())
    attn = _softmax(torch.einsum('bqhd,bkhd->bhqk', q, k) * Dh ** (-0.5), axis=-1)
    a = torch.einsum('bhqk,bkhd->bqhd', attn, v).reshape(B, N, D) @ wo
    x = x + gate_a * a
    h2 = _layernorm(x, axis=-1) * (1 + scale_m) + shift_m
    m = _gelu(h2 @ w_fc1) @ w_fc2
    return x + gate_m * m
```

```cpp
#include <hip/hip_runtime.h>
#include <hip/hip_bf16.h>
typedef unsigned short u16;
__device__ __forceinline__ float bf2f(u16 v){ return __uint_as_float(((unsigned)v)<<16); }
__device__ __forceinline__ u16 f2bf(float f){ unsigned u=__float_as_uint(f); u += 0x7FFFu + ((u>>16)&1u); return (u16)(u>>16); }
constexpr int B=2,N=1024,D=384,F=1536,H=2,DH=192;

__global__ void k_mod(const u16* c, const u16* wmod, float* mod){
  int j = blockIdx.x*blockDim.x+threadIdx.x; int b = blockIdx.y;
  if(j>=6*D) return;
  float acc=0;
  for(int k=0;k<D;k++){ float cv=bf2f(c[b*D+k]); float s=bf2f(f2bf(cv/(1.f+__expf(-cv)))); acc+= s*bf2f(wmod[k*6*D+j]); }
  mod[b*6*D+j]=bf2f(f2bf(acc));
}
__global__ void k_ln(const float* xin_f, const u16* xin, const float* mod, int shift_i, int scale_i, float* h){
  int row=blockIdx.x; int b=row/N; int t=threadIdx.x;
  __shared__ float s[D]; __shared__ float st[2];
  float v = xin? bf2f(xin[row*D+t]) : xin_f[row*D+t];
  s[t]=v; __syncthreads();
  if(t==0){ float m=0; for(int i=0;i<D;i++) m+=s[i]; m/=D; float q=0; for(int i=0;i<D;i++) q+=(s[i]-m)*(s[i]-m); q/=D; st[0]=m; st[1]=rsqrtf(q+1e-5f);}
  __syncthreads();
  float y=(v-st[0])*st[1];
  h[row*D+t]=bf2f(f2bf(y*(1.f+mod[b*6*D+scale_i*D+t])+mod[b*6*D+shift_i*D+t]));
}
__global__ void k_gemm(const float* A, const u16* W, float* C, int K, int Nn, int mode){
  int col=blockIdx.x*blockDim.x+threadIdx.x; int row=blockIdx.y;
  if(col>=Nn) return;
  float acc=0; for(int k=0;k<K;k++) acc+=A[row*K+k]*bf2f(W[k*Nn+col]);
  acc=bf2f(f2bf(acc));
  if(mode==1){ float u=0.7978845608028654f*(acc+0.044715f*acc*acc*acc); acc=bf2f(f2bf(0.5f*acc*(1.f+tanhf(u)))); }
  C[row*Nn+col]=acc;
}
__global__ void k_attn(const float* qkv, float* o){
  int q=blockIdx.x, h=blockIdx.y, b=blockIdx.z; int t=threadIdx.x;
  __shared__ float qs[DH]; __shared__ float p[N]; __shared__ float red[2];
  if(t<DH) qs[t]=qkv[(b*N+q)*3*D+h*DH+t];
  __syncthreads();
  for(int k=t;k<N;k+=blockDim.x){ float a=0; const float* kr=qkv+(b*N+k)*3*D+D+h*DH; for(int d=0;d<DH;d++) a+=qs[d]*kr[d]; p[k]=a*0.07216878364870322f; }
  __syncthreads();
  if(t==0){ float m=-1e30f; for(int k=0;k<N;k++) m=fmaxf(m,p[k]); float s=0; for(int k=0;k<N;k++) s+=__expf(p[k]-m); red[0]=m; red[1]=1.f/s; }
  __syncthreads();
  for(int k=t;k<N;k+=blockDim.x) p[k]=__expf(p[k]-red[0])*red[1];
  __syncthreads();
  if(t<DH){ float a=0; for(int k=0;k<N;k++) a+=p[k]*qkv[(b*N+k)*3*D+2*D+h*DH+t]; o[(b*N+q)*D+h*DH+t]=bf2f(f2bf(a)); }
}
__global__ void k_res(const u16* x, const float* x1in, const float* a, const float* mod, int gate_i, float* x1, u16* out){
  int row=blockIdx.x; int b=row/N; int t=threadIdx.x;
  float xv = x? bf2f(x[row*D+t]) : x1in[row*D+t];
  float r = xv + mod[b*6*D+gate_i*D+t]*a[row*D+t];
  if(x1) x1[row*D+t]=bf2f(f2bf(r)); else out[row*D+t]=f2bf(r);
}
extern "C" void kernel_launch(void* const* d_in, const int* in_sizes, int n_in, void* d_out, int out_size, void* d_ws, size_t ws_size, hipStream_t stream){
  const u16* x=(const u16*)d_in[0]; const u16* c=(const u16*)d_in[1]; const u16* wqkv=(const u16*)d_in[2]; const u16* wo=(const u16*)d_in[3];
  const u16* wmod=(const u16*)d_in[4]; const u16* w1=(const u16*)d_in[5]; const u16* w2=(const u16*)d_in[6];
  float* ws=(float*)d_ws; float* mod=ws; float* h=ws+8192; float* qkv=h+B*N*D; float* o=qkv+B*N*3*D; float* a=o+B*N*D; float* x1=a+B*N*D; float* g=x1+B*N*D; float* m=g+B*N*F;
  k_mod<<<dim3(9,2),256,0,stream>>>(c,wmod,mod);
  k_ln<<<B*N,D,0,stream>>>(nullptr,x,mod,0,1,h);
  k_gemm<<<dim3(9,B*N),128,0,stream>>>(h,wqkv,qkv,D,3*D,0);
  k_attn<<<dim3(N,H,B),256,0,stream>>>(qkv,o);
  k_gemm<<<dim3(3,B*N),128,0,stream>>>(o,wo,a,D,D,0);
  k_res<<<B*N,D,0,stream>>>(x,nullptr,a,mod,2,x1,nullptr);
  k_ln<<<B*N,D,0,stream>>>(x1,nullptr,mod,3,4,h);
  k_gemm<<<dim3(12,B*N),128,0,stream>>>(h,w1,g,D,F,1);
  k_gemm<<<dim3(3,B*N),128,0,stream>>>(g,w2,m,F,D,0);
  k_res<<<B*N,D,0,stream>>>(nullptr,x1,m,mod,5,nullptr,(u16*)d_out);
}
```

```cpp
#include <hip/hip_runtime.h>
#include <hip/hip_bf16.h>
typedef unsigned short u16;
__device__ __forceinline__ float bf2f(u16 v){ return __uint_as_float(((unsigned)v)<<16); }
__device__ __forceinline__ u16 f2bf(float f){ unsigned u=__float_as_uint(f); u += 0x7FFFu + ((u>>16)&1u); return (u16)(u>>16); }
constexpr int B=2,N=1024,D=384,F=1536,H=2,DH=192;

__global__ void k_mod(const u16* c, const u16* wmod, float* mod){
  int j = blockIdx.x*blockDim.x+threadIdx.x; int b = blockIdx.y;
  if(j>=6*D) return;
  float acc=0;
  for(int k=0;k<D;k++){ float cv=bf2f(c[b*D+k]); float s=bf2f(f2bf(cv/(1.f+__expf(-cv)))); acc+= s*bf2f(wmod[k*6*D+j]); }
  mod[b*6*D+j]=bf2f(f2bf(acc));
}
__global__ void k_ln(const float* xin_f, const u16* xin, const float* mod, int shift_i, int scale_i, float* h){
  int row=blockIdx.x; int b=row/N; int t=threadIdx.x;
  __shared__ float s[D]; __shared__ float st[2];
  float v = xin? bf2f(xin[row*D+t]) : xin_f[row*D+t];
  s[t]=v; __syncthreads();
  if(t==0){ float m=0; for(int i=0;i<D;i++) m+=s[i]; m/=D; float q=0; for(int i=0;i<D;i++) q+=(s[i]-m)*(s[i]-m); q/=D; st[0]=m; st[1]=rsqrtf(q+1e-5f);}
  __syncthreads();
  float y=(v-st[0])*st[1];
  h[row*D+t]=bf2f(f2bf(y*(1.f+mod[b*6*D+scale_i*D+t])+mod[b*6*D+shift_i*D+t]));
}
__global__ void k_gemm(const float* A, const u16* W, float* C, int K, int Nn, int mode){
  int col=blockIdx.x*blockDim.x+threadIdx.x; int row=blockIdx.y;
  if(col>=Nn) return;
  float acc=0; for(int k=0;k<K;k++) acc+=A[row*K+k]*bf2f(W[k*Nn+col]);
  acc=bf2f(f2bf(acc));
  if(mode==1){ float u=0.7978845608028654f*(acc+0.044715f*acc*acc*acc); acc=bf2f(f2bf(0.5f*acc*(1.f+tanhf(u)))); }
  C[row*Nn+col]=acc;
}
__global__ void k_attn(const float* qkv, float* o){
  int q=blockIdx.x, h=blockIdx.y, b=blockIdx.z; int t=threadIdx.x;
  __shared__ float qs[DH]; __shared__ float p[N]; __shared__ float red[2];
  if(t<DH) qs[t]=qkv[(b*N+q)*3*D+h*DH+t];
  __syncthreads();
  for(int k=t;k<N;k+=blockDim.x){ float a=0; const float* kr=qkv+(b*N+k)*3*D+D+h*DH; for(int d=0;d<DH;d++) a+=qs[d]*kr[d]; p[k]=a*0.07216878364870322f; }
  __syncthreads();
  if(t==0){ float m=-1e30f; for(int k=0;k<N;k++) m=fmaxf(m,p[k]); float s=0; for(int k=0;k<N;k++) s+=__expf(p[k]-m); red[0]=m; red[1]=1.f/s; }
  __syncthreads();
  for(int k=t;k<N;k+=blockDim.x) p[k]=__expf(p[k]-red[0])*red[1];
  __syncthreads();
  if(t<DH){ float a=0; for(int k=0;k<N;k++) a+=p[k]*qkv[(b*N+k)*3*D+2*D+h*DH+t]; o[(b*N+q)*D+h*DH+t]=bf2f(f2bf(a)); }
}
__global__ void k_res(const u16* x, const float* x1in, const float* a, const float* mod, int gate_i, float* x1, u16* out){
  int row=blockIdx.x; int b=row/N; int t=threadIdx.x;
  float xv = x? bf2f(x[row*D+t]) : x1in[row*D+t];
  float r = xv + mod[b*6*D+gate_i*D+t]*a[row*D+t];
  if(x1) x1[row*D+t]=bf2f(f2bf(r)); else out[row*D+t]=f2bf(r);
}
#define PROBE 0

typedef unsigned v4u __attribute__((ext_vector_type(4)));
template<int REP> __device__ __forceinline__ void body8(float& a,float& b,float& c,float& d){
  #pragma unroll
  for(int i=0;i<REP;i++){
    asm volatile("v_fma_f32 %0, %0, %0, %0\n\tv_fma_f32 %1, %1, %1, %1\n\tv_fma_f32 %2, %2, %2, %2\n\tv_fma_f32 %3, %3, %3, %3\n\t"
                 "v_fma_f32 %0, %0, %0, %0\n\tv_fma_f32 %1, %1, %1, %1\n\tv_fma_f32 %2, %2, %2, %2\n\tv_fma_f32 %3, %3, %3, %3" : "+v"(a),"+v"(b),"+v"(c),"+v"(d));
  }
}
__global__ void __launch_bounds__(256) k_probe(const v4u* x, const v4u* wqkv, const v4u* wmod, const v4u* w1, const v4u* w2, unsigned* sink){
  unsigned long long t0,t1; v4u acc={0,0,0,0}; float a=threadIdx.x,b=1,c=2,d=3;
  t0=0; t1=0;
  if(blockIdx.x==100){
    unsigned long long dur=(t1-t0)*500ull; if(dur>20000000ull) dur=20000000ull;
    unsigned long long s0=__builtin_amdgcn_s_memtime();
    while(__builtin_amdgcn_s_memtime()-s0<dur) __builtin_amdgcn_s_sleep(4);
  }
  if((acc.x^acc.y^acc.z^acc.w)==0x12345678 && a+b+c+d==1.2345f) sink[0]=1;
}
extern "C" void kernel_launch(void* const* d_in, const int* in_sizes, int n_in, void* d_out, int out_size, void* d_ws, size_t ws_size, hipStream_t stream){
  const u16* x=(const u16*)d_in[0]; const u16* c=(const u16*)d_in[1]; const u16* wqkv=(const u16*)d_in[2]; const u16* wo=(const u16*)d_in[3];
  const u16* wmod=(const u16*)d_in[4]; const u16* w1=(const u16*)d_in[5]; const u16* w2=(const u16*)d_in[6];
  float* ws=(float*)d_ws; float* mod=ws; float* h=ws+8192; float* qkv=h+B*N*D; float* o=qkv+B*N*3*D; float* a=o+B*N*D; float* x1=a+B*N*D; float* g=x1+B*N*D; float* m=g+B*N*F;
  k_probe<<<256,256,0,stream>>>((const v4u*)x,(const v4u*)wqkv,(const v4u*)wmod,(const v4u*)w1,(const v4u*)w2,(unsigned*)d_ws);
  k_mod<<<dim3(9,2),256,0,stream>>>(c,wmod,mod);
  k_ln<<<B*N,D,0,stream>>>(nullptr,x,mod,0,1,h);
  k_gemm<<<dim3(9,B*N),128,0,stream>>>(h,wqkv,qkv,D,3*D,0);
  k_attn<<<dim3(N,H,B),256,0,stream>>>(qkv,o);
  k_gemm<<<dim3(3,B*N),128,0,stream>>>(o,wo,a,D,D,0);
  k_res<<<B*N,D,0,stream>>>(x,nullptr,a,mod,2,x1,nullptr);
  k_ln<<<B*N,D,0,stream>>>(x1,nullptr,mod,3,4,h);
  k_gemm<<<dim3(12,B*N),128,0,stream>>>(h,w1,g,D,F,1);
  k_gemm<<<dim3(3,B*N),128,0,stream>>>(g,w2,m,F,D,0);
  k_res<<<B*N,D,0,stream>>>(nullptr,x1,m,mod,5,nullptr,(u16*)d_out);
}
```

```cpp
#include <hip/hip_runtime.h>
#include <hip/hip_bf16.h>
typedef unsigned short u16;
__device__ __forceinline__ float bf2f(u16 v){ return __uint_as_float(((unsigned)v)<<16); }
__device__ __forceinline__ u16 f2bf(float f){ unsigned u=__float_as_uint(f); u += 0x7FFFu + ((u>>16)&1u); return (u16)(u>>16); }
constexpr int B=2,N=1024,D=384,F=1536,H=2,DH=192;

__global__ void k_mod(const u16* c, const u16* wmod, float* mod){
  int j = blockIdx.x*blockDim.x+threadIdx.x; int b = blockIdx.y;
  if(j>=6*D) return;
  float acc=0;
  for(int k=0;k<D;k++){ float cv=bf2f(c[b*D+k]); float s=bf2f(f2bf(cv/(1.f+__expf(-cv)))); acc+= s*bf2f(wmod[k*6*D+j]); }
  mod[b*6*D+j]=bf2f(f2bf(acc));
}
__global__ void k_ln(const float* xin_f, const u16* xin, const float* mod, int shift_i, int scale_i, float* h){
  int row=blockIdx.x; int b=row/N; int t=threadIdx.x;
  __shared__ float s[D]; __shared__ float st[2];
  float v = xin? bf2f(xin[row*D+t]) : xin_f[row*D+t];
  s[t]=v; __syncthreads();
  if(t==0){ float m=0; for(int i=0;i<D;i++) m+=s[i]; m/=D; float q=0; for(int i=0;i<D;i++) q+=(s[i]-m)*(s[i]-m); q/=D; st[0]=m; st[1]=rsqrtf(q+1e-5f);}
  __syncthreads();
  float y=(v-st[0])*st[1];
  h[row*D+t]=bf2f(f2bf(y*(1.f+mod[b*6*D+scale_i*D+t])+mod[b*6*D+shift_i*D+t]));
}
__global__ void k_gemm(const float* A, const u16* W, float* C, int K, int Nn, int mode){
  int col=blockIdx.x*blockDim.x+threadIdx.x; int row=blockIdx.y;
  if(col>=Nn) return;
  float acc=0; for(int k=0;k<K;k++) acc+=A[row*K+k]*bf2f(W[k*Nn+col]);
  acc=bf2f(f2bf(acc));
  if(mode==1){ float u=0.7978845608028654f*(acc+0.044715f*acc*acc*acc); acc=bf2f(f2bf(0.5f*acc*(1.f+tanhf(u)))); }
  C[row*Nn+col]=acc;
}
__global__ void k_attn(const float* qkv, float* o){
  int q=blockIdx.x, h=blockIdx.y, b=blockIdx.z; int t=threadIdx.x;
  __shared__ float qs[DH]; __shared__ float p[N]; __shared__ float red[2];
  if(t<DH) qs[t]=qkv[(b*N+q)*3*D+h*DH+t];
  __syncthreads();
  for(int k=t;k<N;k+=blockDim.x){ float a=0; const float* kr=qkv+(b*N+k)*3*D+D+h*DH; for(int d=0;d<DH;d++) a+=qs[d]*kr[d]; p[k]=a*0.07216878364870322f; }
  __syncthreads();
  if(t==0){ float m=-1e30f; for(int k=0;k<N;k++) m=fmaxf(m,p[k]); float s=0; for(int k=0;k<N;k++) s+=__expf(p[k]-m); red[0]=m; red[1]=1.f/s; }
  __syncthreads();
  for(int k=t;k<N;k+=blockDim.x) p[k]=__expf(p[k]-red[0])*red[1];
  __syncthreads();
  if(t<DH){ float a=0; for(int k=0;k<N;k++) a+=p[k]*qkv[(b*N+k)*3*D+2*D+h*DH+t]; o[(b*N+q)*D+h*DH+t]=bf2f(f2bf(a)); }
}
__global__ void k_res(const u16* x, const float* x1in, const float* a, const float* mod, int gate_i, float* x1, u16* out){
  int row=blockIdx.x; int b=row/N; int t=threadIdx.x;
  float xv = x? bf2f(x[row*D+t]) : x1in[row*D+t];
  float r = xv + mod[b*6*D+gate_i*D+t]*a[row*D+t];
  if(x1) x1[row*D+t]=bf2f(f2bf(r)); else out[row*D+t]=f2bf(r);
}
#define PROBE 4

typedef unsigned v4u __attribute__((ext_vector_type(4)));
template<int REP> __device__ __forceinline__ void body8(float& a,float& b,float& c,float& d){
  #pragma unroll
  for(int i=0;i<REP;i++){
    asm volatile("v_fma_f32 %0, %0, %0, %0\n\tv_fma_f32 %1, %1, %1, %1\n\tv_fma_f32 %2, %2, %2, %2\n\tv_fma_f32 %3, %3, %3, %3\n\t"
                 "v_fma_f32 %0, %0, %0, %0\n\tv_fma_f32 %1, %1, %1, %1\n\tv_fma_f32 %2, %2, %2, %2\n\tv_fma_f32 %3, %3, %3, %3" : "+v"(a),"+v"(b),"+v"(c),"+v"(d));
  }
}
#define STAMP(t) do{ __builtin_amdgcn_sched_barrier(0); asm volatile("s_memtime %0\n\ts_waitcnt lgkmcnt(0)" : "=s"(t) :: "memory"); __builtin_amdgcn_sched_barrier(0);}while(0)
__global__ void __launch_bounds__(256) k_probe(const v4u* x, const v4u* wqkv, const v4u* wmod, const v4u* w1, const v4u* w2, unsigned* sink){
  unsigned long long t0,t1,tm; v4u acc={0,0,0,0}; float a=threadIdx.x,b=1,c=2,d=3;
  STAMP(t0);
  asm volatile("" :: "s"(wmod));
  STAMP(t1);
  acc = wmod[blockIdx.x*256+threadIdx.x];
  if(blockIdx.x==100){
    unsigned long long dur=(t1-t0)*500ull; if(dur>20000000ull) dur=20000000ull;
    unsigned long long s0=__builtin_amdgcn_s_memtime();
    while(__builtin_amdgcn_s_memtime()-s0<dur) __builtin_amdgcn_s_sleep(4);
  }
  if((acc.x^acc.y^acc.z^acc.w)==0x12345678 && a+b+c+d==1.2345f) sink[0]=1;
}
extern "C" void kernel_launch(void* const* d_in, const int* in_sizes, int n_in, void* d_out, int out_size, void* d_ws, size_t ws_size, hipStream_t stream){
  const u16* x=(const u16*)d_in[0]; const u16* c=(const u16*)d_in[1]; const u16* wqkv=(const u16*)d_in[2]; const u16* wo=(const u16*)d_in[3];
  const u16* wmod=(const u16*)d_in[4]; const u16* w1=(const u16*)d_in[5]; const u16* w2=(const u16*)d_in[6];
  float* ws=(float*)d_ws; float* mod=ws; float* h=ws+8192; float* qkv=h+B*N*D; float* o=qkv+B*N*3*D; float* a=o+B*N*D; float* x1=a+B*N*D; float* g=x1+B*N*D; float* m=g+B*N*F;
  k_probe<<<256,256,0,stream>>>((const v4u*)x,(const v4u*)wqkv,(const v4u*)wmod,(const v4u*)w1,(const v4u*)w2,(unsigned*)d_ws);
  k_mod<<<dim3(9,2),256,0,stream>>>(c,wmod,mod);
  k_ln<<<B*N,D,0,stream>>>(nullptr,x,mod,0,1,h);
  k_gemm<<<dim3(9,B*N),128,0,stream>>>(h,wqkv,qkv,D,3*D,0);
  k_attn<<<dim3(N,H,B),256,0,stream>>>(qkv,o);
  k_gemm<<<dim3(3,B*N),128,0,stream>>>(o,wo,a,D,D,0);
  k_res<<<B*N,D,0,stream>>>(x,nullptr,a,mod,2,x1,nullptr);
  k_ln<<<B*N,D,0,stream>>>(x1,nullptr,mod,3,4,h);
  k_gemm<<<dim3(12,B*N),128,0,stream>>>(h,w1,g,D,F,1);
  k_gemm<<<dim3(3,B*N),128,0,stream>>>(g,w2,m,F,D,0);
  k_res<<<B*N,D,0,stream>>>(nullptr,x1,m,mod,5,nullptr,(u16*)d_out);
}
```

```cpp
#include <hip/hip_runtime.h>
#include <hip/hip_bf16.h>

typedef unsigned short u16;
typedef unsigned long long u64;
#define LAS __attribute__((address_space(3)))

#define WS_SHIFT(n) ((size_t)0)
constexpr unsigned WS_BYTES = 27164672u;
constexpr int OFF_STAMP = 26771456;
constexpr int LDS_BYTES = 163392;

__global__ void __launch_bounds__(768, 3)
dit_block(const void* p0, unsigned n0, unsigned f0, const void* p1, unsigned n1, unsigned f1, const void* p2, unsigned n2, unsigned f2, const void* p3, unsigned n3, unsigned f3, const void* p4, unsigned n4, unsigned f4, const void* p5, unsigned n5, unsigned f5, const void* p6, unsigned n6, unsigned f6, const void* p7, unsigned n7, unsigned f7, const void* p8, unsigned n8, unsigned f8) {
  __shared__ __attribute__((aligned(16))) char smem[LDS_BYTES];
  asm volatile("" ::"s"((unsigned)(size_t)(LAS char*)smem));
  unsigned tid = threadIdx.x, bid = blockIdx.x;
  u64 ka = (u64)__builtin_amdgcn_kernarg_segment_ptr();
  asm volatile(
    "s_load_dwordx16 s[8:23], %1, 0x0\n"
    "s_load_dwordx16 s[24:39], %1, 0x40\n"
    "s_load_dwordx4 s[40:43], %1, 0x80\n"
    "v_readfirstlane_b32 s3, v0\n"
    "v_and_b32 v1, 63, v0\n"
    "s_and_b32 s4, %2, 7\n"
    "s_lshr_b32 s5, %2, 3\n"
    "s_lshr_b32 s3, s3, 6\n"
    "s_and_b32 s6, s5, 3\n"
    "s_lshr_b32 s7, s5, 2\n"
    "s_cmp_lg_u32 s3, 11\n"
    "s_cbranch_scc1 L_nopf\n"
    "s_getpc_b64 s[56:57]\n"
    "s_and_b32 s56, s56, 0xffffff00\n"
    "s_min_u32 s58, s5, 19\n"
    "s_lshl_b32 s58, s58, 10\n"
    "v_lshl_add_u32 v7, v1, 4, s58\n"
    "global_load_dword v167, v7, s[56:57]\n"
    "L_nopf:\n"
    "v_lshrrev_b32 v5, 3, v1\n"
    "v_and_b32 v6, 7, v1\n"
    "s_cmp_eq_u32 s6, 3\n"
    "s_cbranch_scc1 L_help0\n"
    "s_cmp_lt_u32 s3, 4\n"
    "s_cbranch_scc1 L_kw0\n"
    "s_lshr_b32 s57, s4, 1\n"
    "s_lshl_b32 s57, s57, 3\n"
    "s_add_u32 s57, s57, s7\n"
    "s_lshl_b32 s57, s57, 3\n"
    "s_add_u32 s57, s57, s3\n"
    "s_sub_u32 s57, s57, 4\n"
    "s_mul_i32 s57, s57, 6144\n"
    "v_mul_u32_u24 v7, 768, v5\n"
    "v_lshl_add_u32 v7, v6, 4, v7\n"
    "s_setprio 3\n"
    "s_waitcnt lgkmcnt(0)\n"
    "buffer_load_dwordx4 v[8:11], v7, s[8:11], s57 offen offset:0\n"
    "buffer_load_dwordx4 v[12:15], v7, s[8:11], s57 offen offset:128\n"
    "buffer_load_dwordx4 v[16:19], v7, s[8:11], s57 offen offset:256\n"
    "buffer_load_dwordx4 v[20:23], v7, s[8:11], s57 offen offset:384\n"
    "buffer_load_dwordx4 v[24:27], v7, s[8:11], s57 offen offset:512\n"
    "buffer_load_dwordx4 v[28:31], v7, s[8:11], s57 offen offset:640\n"
    "buffer_load_dword v166, off, s[40:43], 0 sc1\n"
    "s_setprio 0\n"
    "s_branch L_ln1\n"
    "L_kw0:\n"
    "v_bfe_u32 v7, v1, 4, 1\n"
    "v_lshlrev_b32 v7, 2, v7\n"
    "v_xor_b32 v7, v6, v7\n"
    "v_mul_u32_u24 v163, 2304, v5\n"
    "v_lshl_add_u32 v163, v7, 4, v163\n"
    "v_add_u32 v164, 17408, v163\n"
    "v_add_u32 v165, 34816, v163\n"
    "s_and_b32 s56, s4, 1\n"
    "s_mul_i32 s56, s56, 384\n"
    "s_lshl_b32 s57, s6, 7\n"
    "s_add_u32 s56, s56, s57\n"
    "s_mul_i32 s57, s3, 221184\n"
    "s_add_u32 s56, s56, s57\n"
    "s_mul_i32 s57, s3, 12288\n"
    "s_waitcnt lgkmcnt(0)\n"
    "s_add_u32 s58, s7, 0\n"
    "s_and_b32 s58, s58, 3\n"
    "s_mul_i32 s24, s58, 55296\n"
    "s_mul_i32 s28, s58, 3072\n"
    "s_add_u32 s28, s28, s57\n"
    "s_mov_b32 m0, s28\n"
    "s_add_u32 s24, s24, s56\n"
    "buffer_load_dwordx4 v163, s[16:19], s24 offen offset:0 lds\n"
    "buffer_load_dwordx4 v164, s[16:19], s24 offen offset:1024 lds\n"
    "buffer_load_dwordx4 v165, s[16:19], s24 offen offset:2048 lds\n"
    "s_add_u32 s58, s7, 1\n"
    "s_and_b32 s58, s58, 3\n"
    "s_mul_i32 s25, s58, 55296\n"
    "s_mul_i32 s29, s58, 3072\n"
    "s_add_u32 s29, s29, s57\n"
    "s_mov_b32 m0, s29\n"
    "s_add_u32 s25, s25, s56\n"
    "buffer_load_dwordx4 v163, s[16:19], s25 offen offset:0 lds\n"
    "buffer_load_dwordx4 v164, s[16:19], s25 offen offset:1024 lds\n"
    "buffer_load_dwordx4 v165, s[16:19], s25 offen offset:2048 lds\n"
    "s_add_u32 s58, s7, 2\n"
    "s_and_b32 s58, s58, 3\n"
    "s_mul_i32 s26, s58, 55296\n"
    "s_mul_i32 s30, s58, 3072\n"
    "s_add_u32 s30, s30, s57\n"
    "s_mov_b32 m0, s30\n"
    "s_add_u32 s26, s26, s56\n"
    "buffer_load_dwordx4 v163, s[16:19], s26 offen offset:0 lds\n"
    "buffer_load_dwordx4 v164, s[16:19], s26 offen offset:1024 lds\n"
    "buffer_load_dwordx4 v165, s[16:19], s26 offen offset:2048 lds\n"
    "s_add_u32 s58, s7, 3\n"
    "s_and_b32 s58, s58, 3\n"
    "s_mul_i32 s27, s58, 55296\n"
    "s_mul_i32 s31, s58, 3072\n"
    "s_add_u32 s31, s31, s57\n"
    "s_mov_b32 m0, s31\n"
    "s_add_u32 s27, s27, s56\n"
    "buffer_load_dwordx4 v163, s[16:19], s27 offen offset:0 lds\n"
    "buffer_load_dwordx4 v164, s[16:19], s27 offen offset:1024 lds\n"
    "buffer_load_dwordx4 v165, s[16:19], s27 offen offset:2048 lds\n"
    "s_branch L_kw1\n"
    "L_help0:\n"
    "v_bfe_u32 v7, v1, 4, 1\n"
    "v_lshlrev_b32 v7, 2, v7\n"
    "v_xor_b32 v7, v6, v7\n"
    "s_lshl_b32 s59, s7, 3\n"
    "s_add_u32 s59, s59, s4\n"
    "s_lshl_b32 s2, s3, 13\n"
    "s_cmp_ge_u32 s59, 12\n"
    "s_cbranch_scc1 L_help1\n"
    "v_mul_u32_u24 v8, 4608, v5\n"
    "v_lshl_add_u32 v8, v7, 4, v8\n"
    "v_add_u32 v9, 35840, v8\n"
    "v_add_u32 v10, 71680, v8\n"
    "v_add_u32 v11, 107520, v8\n"
    "v_lshrrev_b32 v12, 5, v1\n"
    "v_mul_u32_u24 v12, 704, v12\n"
    "v_lshl_add_u32 v12, v1, 1, v12\n"
    "s_mul_i32 s56, s3, 147456\n"
    "s_lshl_b32 s57, s59, 7\n"
    "s_add_u32 s56, s56, s57\n"
    "s_lshl_b32 s57, s3, 6\n"
    "s_add_u32 m0, s2, 4096\n"
    "s_waitcnt lgkmcnt(0)\n"
    "buffer_load_ushort v13, v12, s[12:15], s57 offen\n"
    "buffer_load_dword v166, off, s[40:43], 0 sc1\n"
    "buffer_load_dwordx4 v8, s[24:27], s56 offen offset:0 lds\n"
    "buffer_load_dwordx4 v9, s[24:27], s56 offen offset:1024 lds\n"
    "buffer_load_dwordx4 v10, s[24:27], s56 offen offset:2048 lds\n"
    "buffer_load_dwordx4 v11, s[24:27], s56 offen offset:3072 lds\n"
    "v_bfe_u32 v18, v1, 2, 2\n"
    "v_lshrrev_b32 v19, 1, v18\n"
    "v_lshlrev_b32 v18, 7, v18\n"
    "v_lshl_add_u32 v18, v19, 6, v18\n"
    "v_bfe_u32 v19, v1, 4, 1\n"
    "v_lshl_add_u32 v18, v19, 5, v18\n"
    "v_and_b32 v19, 3, v1\n"
    "v_lshl_add_u32 v18, v19, 3, v18\n"
    "v_lshrrev_b32 v19, 5, v1\n"
    "v_lshl_add_u32 v18, v19, 9, v18\n"
    "v_add_u32 v18, s2, v18\n"
    "v_xor_b32 v19, 64, v18\n"
    "s_lshl_b32 s56, s3, 7\n"
    "s_add_u32 s56, s56, 98304\n"
    "v_lshl_add_u32 v14, v1, 1, s56\n"
    "v_and_b32 v15, 1, v1\n"
    "v_lshrrev_b32 v16, 5, v1\n"
    "v_lshl_add_u32 v15, v15, 3, v16\n"
    "v_lshl_add_u32 v15, v15, 3, s56\n"
    "s_waitcnt vmcnt(5)\n"
    "v_lshlrev_b32 v13, 16, v13\n"
    "v_mul_f32 v17, 0xbfb8aa3b, v13\n"
    "v_exp_f32 v17, v17\n"
    "s_nop 0\n"
    "v_add_f32 v17, 1.0, v17\n"
    "v_rcp_f32 v17, v17\n"
    "s_nop 0\n"
    "v_mul_f32 v13, v13, v17\n"
    "v_cvt_pk_bf16_f32 v13, v13, v13\n"
    "ds_write_b16 v14, v13\n"
    "ds_read_b64 v[24:25], v15 offset:0\n"
    "ds_read_b64 v[26:27], v15 offset:16\n"
    "ds_read_b64 v[28:29], v15 offset:32\n"
    "ds_read_b64 v[30:31], v15 offset:48\n"
    "s_lshl_b32 s56, s3, 9\n"
    "s_add_u32 s56, s56, 100352\n"
    "v_lshl_add_u32 v20, v1, 2, s56\n"
    "v_lshlrev_b32 v21, 3, v1\n"
    "v_mul_u32_u24 v22, 2816, v16\n"
    "v_add_u32 v22, v21, v22\n"
    "v_add_u32 v21, 100352, v21\n"
    "s_lshl_b32 s57, s59, 8\n"
    "s_mul_i32 s58, s3, 6144\n"
    "s_add_u32 s57, s57, s58\n"
    "s_add_u32 s57, s57, 274432\n"
    "s_sub_u32 s56, s59, 6\n"
    "s_cmp_lt_u32 s56, 6\n"
    "s_cselect_b32 s58, 1.0, 0\n"
    "s_waitcnt vmcnt(4)\n"
    "v_readfirstlane_b32 s44, v166\n"
    "s_mul_i32 s44, s44, 1664525\n"
    "s_add_u32 s44, s44, 1013904223\n"
    "s_not_b32 s45, s44\n"
    "v_mov_b32 v2, s44\n"
    "v_mov_b32 v3, s45\n"
    "s_waitcnt vmcnt(0)\n"
    "ds_read_b64_tr_b16 v[32:33], v18 offset:4096\n"
    "ds_read_b64_tr_b16 v[34:35], v18 offset:5120\n"
    "ds_read_b64_tr_b16 v[36:37], v19 offset:4096\n"
    "ds_read_b64_tr_b16 v[38:39], v19 offset:5120\n"
    "ds_read_b64_tr_b16 v[40:41], v18 offset:6144\n"
    "ds_read_b64_tr_b16 v[42:43], v18 offset:7168\n"
    "ds_read_b64_tr_b16 v[44:45], v19 offset:6144\n"
    "ds_read_b64_tr_b16 v[46:47], v19 offset:7168\n"
    "s_waitcnt lgkmcnt(6)\n"
    "v_mfma_f32_32x32x16_bf16 v[56:71], v[24:27], v[32:35], 0\n"
    "s_waitcnt lgkmcnt(4)\n"
    "v_mfma_f32_32x32x16_bf16 v[72:87], v[24:27], v[36:39], 0\n"
    "s_waitcnt lgkmcnt(2)\n"
    "v_mfma_f32_32x32x16_bf16 v[56:71], v[28:31], v[40:43], v[56:71]\n"
    "s_waitcnt lgkmcnt(0)\n"
    "v_mfma_f32_32x32x16_bf16 v[72:87], v[28:31], v[44:47], v[72:87]\n"
    "s_mov_b32 exec_hi, 0\n"
    "s_nop 8\n"
    "ds_write_b32 v20, v56\n"
    "ds_write_b32 v20, v57 offset:256\n"
    "ds_write_b32 v20, v72 offset:128\n"
    "ds_write_b32 v20, v73 offset:384\n"
    "s_mov_b32 exec_hi, -1\n"
    "s_waitcnt lgkmcnt(0)\n"
    "s_barrier\n"
    "s_cmp_ge_u32 s3, 8\n"
    "s_cbranch_scc1 L_modend_1\n"
    "ds_read_b64 v[24:25], v21 offset:0\n"
    "ds_read_b64 v[26:27], v21 offset:512\n"
    "ds_read_b64 v[28:29], v21 offset:1024\n"
    "ds_read_b64 v[30:31], v21 offset:1536\n"
    "ds_read_b64 v[32:33], v21 offset:2048\n"
    "ds_read_b64 v[34:35], v21 offset:2560\n"
    "ds_read_b64 v[36:37], v21 offset:3072\n"
    "ds_read_b64 v[38:39], v21 offset:3584\n"
    "ds_read_b64 v[40:41], v21 offset:4096\n"
    "ds_read_b64 v[42:43], v21 offset:4608\n"
    "ds_read_b64 v[44:45], v21 offset:5120\n"
    "ds_read_b64 v[46:47], v21 offset:5632\n"
    "s_waitcnt lgkmcnt(10)\n"
    "v_pk_add_f32 v[24:25], v[24:25], v[26:27]\n"
    "s_waitcnt lgkmcnt(8)\n"
    "v_pk_add_f32 v[28:29], v[28:29], v[30:31]\n"
    "s_waitcnt lgkmcnt(6)\n"
    "v_pk_add_f32 v[32:33], v[32:33], v[34:35]\n"
    "s_waitcnt lgkmcnt(4)\n"
    "v_pk_add_f32 v[36:37], v[36:37], v[38:39]\n"
    "s_waitcnt lgkmcnt(2)\n"
    "v_pk_add_f32 v[40:41], v[40:41], v[42:43]\n"
    "s_waitcnt lgkmcnt(0)\n"
    "v_pk_add_f32 v[44:45], v[44:45], v[46:47]\n"
    "v_pk_add_f32 v[24:25], v[24:25], v[28:29]\n"
    "v_pk_add_f32 v[32:33], v[32:33], v[36:37]\n"
    "v_pk_add_f32 v[40:41], v[40:41], v[44:45]\n"
    "v_pk_add_f32 v[24:25], v[24:25], v[32:33]\n"
    "v_pk_add_f32 v[24:25], v[24:25], v[40:41]\n"
    "v_cvt_pk_bf16_f32 v28, v24, v25\n"
    "v_lshlrev_b32 v24, 16, v28\n"
    "v_and_b32 v25, 0xffff0000, v28\n"
    "v_add_f32 v24, s58, v24\n"
    "v_add_f32 v25, s58, v25\n"
    "v_cvt_pk_bf16_f32 v28, v24, v25\n"
    "v_mov_b32 v29, s44\n"
    "buffer_store_dwordx2 v[28:29], v22, s[40:43], s57 offen sc1\n"
    "L_modend_1:\n"
    "s_branch L_crit1\n"
    "L_ln1:\n"
    "v_mov_b32 v140, 0\n"
    "v_mov_b32 v141, 0\n"
    "v_mov_b32 v142, 0\n"
    "v_mov_b32 v143, 0\n"
    "v_mov_b32 v144, 163328\n"
    "ds_write_b64 v144, v[140:141]\n"
    "s_and_b32 s57, s3, 3\n"
    "s_lshl_b32 s57, s57, 3\n"
    "v_add_u32 v148, s57, v5\n"
    "v_xor_b32 v148, v148, v6\n"
    "v_and_b32 v149, 1, v6\n"
    "v_lshl_add_u32 v148, v149, 5, v148\n"
    "v_lshrrev_b32 v149, 1, v6\n"
    "v_lshl_add_u32 v148, v149, 6, v148\n"
    "s_lshr_b32 s56, s3, 3\n"
    "s_mul_i32 s56, s56, 24576\n"
    "v_lshl_add_u32 v148, v148, 4, s56\n"
    "v_lshlrev_b32 v149, 5, v6\n"
    "v_add_u32 v149, 161792, v149\n"
    "v_bfe_u32 v7, v1, 4, 1\n"
    "v_lshlrev_b32 v7, 2, v7\n"
    "v_xor_b32 v7, v6, v7\n"
    "v_mul_u32_u24 v163, 2304, v5\n"
    "v_lshl_add_u32 v163, v7, 4, v163\n"
    "v_add_u32 v164, 17408, v163\n"
    "v_add_u32 v165, 34816, v163\n"
    "s_and_b32 s24, s4, 1\n"
    "s_mul_i32 s24, s24, 384\n"
    "s_lshl_b32 s25, s6, 7\n"
    "s_add_u32 s24, s24, s25\n"
    "s_and_b32 s25, s3, 3\n"
    "s_mul_i32 s26, s25, 221184\n"
    "s_add_u32 s24, s24, s26\n"
    "s_add_u32 s24, s24, 1536\n"
    "s_mul_i32 s25, s25, 12288\n"
    "s_add_u32 s25, s25, 98304\n"
    "s_movk_i32 s47, 4096\n"
    "s_movk_i32 s48, 8192\n"
    "s_movk_i32 s49, 12288\n"
    "s_movk_i32 s50, 16384\n"
    "s_movk_i32 s51, 20480\n"
    "v_lshlrev_b32 v0, 4, v1\n"
    "s_mul_i32 s56, s3, 24576\n"
    "s_add_u32 s56, s56, 24117248\n"
    "v_add_u32 v4, s56, v0\n"
    "s_and_b32 s56, s5, 1\n"
    "s_lshl_b32 s57, s4, 4\n"
    "s_lshl_b32 s58, s56, 3\n"
    "s_or_b32 s57, s57, s58\n"
    "s_lshr_b32 s58, s3, 1\n"
    "s_or_b32 s58, s58, s57\n"
    "s_lshl_b32 s58, s58, 7\n"
    "s_add_u32 s60, s58, 49152\n"
    "s_add_u32 s57, s57, s3\n"
    "s_and_b32 s58, s4, 5\n"
    "s_lshl_b32 s58, s58, 4\n"
    "s_and_b32 s0, s5, 16\n"
    "s_lshl_b32 s0, s0, 1\n"
    "s_or_b32 s58, s58, s0\n"
    "s_and_b32 s0, s5, 14\n"
    "s_or_b32 s58, s58, s0\n"
    "s_bfe_u32 s0, s3, 0x10001\n"
    "s_or_b32 s58, s58, s0\n"
    "s_and_b32 s0, s3, 1\n"
    "s_mul_i32 s1, s0, 6144\n"
    "s_add_u32 s1, s1, 3145728\n"
    "s_mul_i32 s0, s0, 24\n"
    "s_add_u32 s0, s0, 32768\n"
    "s_lshl_b32 s34, s58, 14\n"
    "s_add_u32 s34, s34, s1\n"
    "s_lshl_b32 s35, s58, 7\n"
    "s_add_u32 s35, s35, s0\n"
    "s_lshl_b32 s58, s57, 14\n"
    "s_add_u32 s54, s58, 1048576\n"
    "s_lshl_b32 s58, s57, 7\n"
    "s_add_u32 s55, s58, 16384\n"
    "s_and_b32 s57, s4, 4\n"
    "s_lshl_b32 s57, s57, 2\n"
    "s_lshr_b32 s58, s5, 1\n"
    "s_or_b32 s57, s57, s58\n"
    "s_and_b32 s58, s4, 1\n"
    "s_lshl_b32 s58, s58, 2\n"
    "s_and_b32 s0, s4, 2\n"
    "s_or_b32 s58, s58, s0\n"
    "s_or_b32 s58, s58, s56\n"
    "s_lshl_b32 s0, s57, 4\n"
    "s_or_b32 s1, s0, s58\n"
    "s_lshl_b32 s1, s1, 4\n"
    "s_or_b32 s1, s1, s3\n"
    "s_lshl_b32 s1, s1, 10\n"
    "s_add_u32 s61, s1, 7340032\n"
    "s_or_b32 s0, s0, s3\n"
    "s_lshl_b32 s0, s0, 4\n"
    "s_or_b32 s0, s0, s58\n"
    "s_lshl_b32 s0, s0, 3\n"
    "s_add_u32 s62, s0, 65536\n"
    "s_lshl_b32 s57, s4, 3\n"
    "s_or_b32 s57, s57, s7\n"
    "s_mul_i32 s58, s57, 24576\n"
    "s_lshl_b32 s0, s3, 6\n"
    "s_add_u32 s63, s58, s0\n"
    "s_lshr_b32 s58, s4, 2\n"
    "s_mul_i32 s58, s58, 6144\n"
    "s_lshl_b32 s0, s3, 9\n"
    "s_add_u32 s58, s58, s0\n"
    "s_add_u32 s96, s58, 323584\n"
    "s_lshl_b32 s58, s57, 7\n"
    "s_or_b32 s58, s58, s3\n"
    "s_lshl_b32 s58, s58, 10\n"
    "s_add_u32 s97, s58, 7340032\n"
    "s_lshr_b32 s58, s57, 1\n"
    "s_lshl_b32 s0, s58, 4\n"
    "s_or_b32 s0, s0, s3\n"
    "s_lshl_b32 s0, s0, 7\n"
    "s_add_u32 s98, s0, 65536\n"
    "s_lshl_b32 s58, s58, 4\n"
    "s_and_b32 s0, s57, 1\n"
    "s_or_b32 s58, s58, s0\n"
    "s_lshl_b32 s58, s58, 8\n"
    "s_add_u32 s99, s58, 393216\n"
    "s_mov_b32 s56, 0x3f803f80\n"
    "s_waitcnt vmcnt(6)\n"
    "v_dot2c_f32_bf16 v140, s56, v8\n"
    "v_dot2c_f32_bf16 v141, v8, v8\n"
    "v_dot2c_f32_bf16 v142, s56, v9\n"
    "v_dot2c_f32_bf16 v143, v9, v9\n"
    "v_dot2c_f32_bf16 v140, s56, v10\n"
    "v_dot2c_f32_bf16 v141, v10, v10\n"
    "v_dot2c_f32_bf16 v142, s56, v11\n"
    "v_dot2c_f32_bf16 v143, v11, v11\n"
    "s_waitcnt vmcnt(5)\n"
    "v_dot2c_f32_bf16 v140, s56, v12\n"
    "v_dot2c_f32_bf16 v141, v12, v12\n"
    "v_dot2c_f32_bf16 v142, s56, v13\n"
    "v_dot2c_f32_bf16 v143, v13, v13\n"
    "v_dot2c_f32_bf16 v140, s56, v14\n"
    "v_dot2c_f32_bf16 v141, v14, v14\n"
    "v_dot2c_f32_bf16 v142, s56, v15\n"
    "v_dot2c_f32_bf16 v143, v15, v15\n"
    "s_waitcnt vmcnt(4)\n"
    "v_dot2c_f32_bf16 v140, s56, v16\n"
    "v_dot2c_f32_bf16 v141, v16, v16\n"
    "v_dot2c_f32_bf16 v142, s56, v17\n"
    "v_dot2c_f32_bf16 v143, v17, v17\n"
    "v_dot2c_f32_bf16 v140, s56, v18\n"
    "v_dot2c_f32_bf16 v141, v18, v18\n"
    "v_dot2c_f32_bf16 v142, s56, v19\n"
    "v_dot2c_f32_bf16 v143, v19, v19\n"
    "s_waitcnt vmcnt(3)\n"
    "v_dot2c_f32_bf16 v140, s56, v20\n"
    "v_dot2c_f32_bf16 v141, v20, v20\n"
    "v_dot2c_f32_bf16 v142, s56, v21\n"
    "v_dot2c_f32_bf16 v143, v21, v21\n"
    "v_dot2c_f32_bf16 v140, s56, v22\n"
    "v_dot2c_f32_bf16 v141, v22, v22\n"
    "v_dot2c_f32_bf16 v142, s56, v23\n"
    "v_dot2c_f32_bf16 v143, v23, v23\n"
    "s_waitcnt vmcnt(2)\n"
    "v_dot2c_f32_bf16 v140, s56, v24\n"
    "v_dot2c_f32_bf16 v141, v24, v24\n"
    "v_dot2c_f32_bf16 v142, s56, v25\n"
    "v_dot2c_f32_bf16 v143, v25, v25\n"
    "v_dot2c_f32_bf16 v140, s56, v26\n"
    "v_dot2c_f32_bf16 v141, v26, v26\n"
    "v_dot2c_f32_bf16 v142, s56, v27\n"
    "v_dot2c_f32_bf16 v143, v27, v27\n"
    "s_waitcnt vmcnt(1)\n"
    "v_dot2c_f32_bf16 v140, s56, v28\n"
    "v_dot2c_f32_bf16 v141, v28, v28\n"
    "v_dot2c_f32_bf16 v142, s56, v29\n"
    "v_dot2c_f32_bf16 v143, v29, v29\n"
    "v_dot2c_f32_bf16 v140, s56, v30\n"
    "v_dot2c_f32_bf16 v141, v30, v30\n"
    "v_dot2c_f32_bf16 v142, s56, v31\n"
    "v_dot2c_f32_bf16 v143, v31, v31\n"
    "s_nop 1\n"
    "v_add_f32 v140, v140, v142\n"
    "v_add_f32 v141, v141, v143\n"
    "s_nop 0\n"
    "v_add_f32_dpp v140, v140, v140 quad_perm:[1,0,3,2] row_mask:0xf bank_mask:0xf\n"
    "v_add_f32_dpp v141, v141, v141 quad_perm:[1,0,3,2] row_mask:0xf bank_mask:0xf\n"
    "s_nop 0\n"
    "v_add_f32_dpp v140, v140, v140 quad_perm:[2,3,0,1] row_mask:0xf bank_mask:0xf\n"
    "v_add_f32_dpp v141, v141, v141 quad_perm:[2,3,0,1] row_mask:0xf bank_mask:0xf\n"
    "s_nop 0\n"
    "v_add_f32_dpp v140, v140, v140 row_half_mirror row_mask:0xf bank_mask:0xf\n"
    "v_add_f32_dpp v141, v141, v141 row_half_mirror row_mask:0xf bank_mask:0xf\n"
    "v_mul_f32 v140, 0x3b2aaaab, v140\n"
    "v_mul_f32 v141, 0x3b2aaaab, v141\n"
    "v_fma_f32 v141, -v140, v140, v141\n"
    "v_max_f32 v141, 0, v141\n"
    "v_add_f32 v141, 0x3727c5ac, v141\n"
    "v_rsq_f32 v142, v141\n"
    "s_nop 0\n"
    "v_mul_f32 v144, v140, v142\n"
    "v_lshlrev_b32 v156, 16, v8\n"
    "v_and_b32 v157, 0xffff0000, v8\n"
    "v_pk_fma_f32 v[156:157], v[156:157], v[142:143], v[144:145] op_sel_hi:[1,0,0] neg_lo:[0,0,1] neg_hi:[0,0,1]\n"
    "v_cvt_pk_bf16_f32 v40, v156, 1.0\n"
    "v_cvt_pk_bf16_f32 v41, v157, 1.0\n"
    "v_lshlrev_b32 v158, 16, v9\n"
    "v_and_b32 v159, 0xffff0000, v9\n"
    "v_pk_fma_f32 v[158:159], v[158:159], v[142:143], v[144:145] op_sel_hi:[1,0,0] neg_lo:[0,0,1] neg_hi:[0,0,1]\n"
    "v_cvt_pk_bf16_f32 v42, v158, 1.0\n"
    "v_cvt_pk_bf16_f32 v43, v159, 1.0\n"
    "v_lshlrev_b32 v156, 16, v10\n"
    "v_and_b32 v157, 0xffff0000, v10\n"
    "v_pk_fma_f32 v[156:157], v[156:157], v[142:143], v[144:145] op_sel_hi:[1,0,0] neg_lo:[0,0,1] neg_hi:[0,0,1]\n"
    "v_cvt_pk_bf16_f32 v44, v156, 1.0\n"
    "v_cvt_pk_bf16_f32 v45, v157, 1.0\n"
    "v_lshlrev_b32 v158, 16, v11\n"
    "v_and_b32 v159, 0xffff0000, v11\n"
    "v_pk_fma_f32 v[158:159], v[158:159], v[142:143], v[144:145] op_sel_hi:[1,0,0] neg_lo:[0,0,1] neg_hi:[0,0,1]\n"
    "v_cvt_pk_bf16_f32 v46, v158, 1.0\n"
    "v_cvt_pk_bf16_f32 v47, v159, 1.0\n"
    "v_lshlrev_b32 v156, 16, v12\n"
    "v_and_b32 v157, 0xffff0000, v12\n"
    "v_pk_fma_f32 v[156:157], v[156:157], v[142:143], v[144:145] op_sel_hi:[1,0,0] neg_lo:[0,0,1] neg_hi:[0,0,1]\n"
    "v_cvt_pk_bf16_f32 v48, v156, 1.0\n"
    "v_cvt_pk_bf16_f32 v49, v157, 1.0\n"
    "v_lshlrev_b32 v158, 16, v13\n"
    "v_and_b32 v159, 0xffff0000, v13\n"
    "v_pk_fma_f32 v[158:159], v[158:159], v[142:143], v[144:145] op_sel_hi:[1,0,0] neg_lo:[0,0,1] neg_hi:[0,0,1]\n"
    "v_cvt_pk_bf16_f32 v50, v158, 1.0\n"
    "v_cvt_pk_bf16_f32 v51, v159, 1.0\n"
    "v_lshlrev_b32 v156, 16, v14\n"
    "v_and_b32 v157, 0xffff0000, v14\n"
    "v_pk_fma_f32 v[156:157], v[156:157], v[142:143], v[144:145] op_sel_hi:[1,0,0] neg_lo:[0,0,1] neg_hi:[0,0,1]\n"
    "v_cvt_pk_bf16_f32 v52, v156, 1.0\n"
    "v_cvt_pk_bf16_f32 v53, v157, 1.0\n"
    "v_lshlrev_b32 v158, 16, v15\n"
    "v_and_b32 v159, 0xffff0000, v15\n"
    "v_pk_fma_f32 v[158:159], v[158:159], v[142:143], v[144:145] op_sel_hi:[1,0,0] neg_lo:[0,0,1] neg_hi:[0,0,1]\n"
    "v_cvt_pk_bf16_f32 v54, v158, 1.0\n"
    "v_cvt_pk_bf16_f32 v55, v159, 1.0\n"
    "v_lshlrev_b32 v156, 16, v16\n"
    "v_and_b32 v157, 0xffff0000, v16\n"
    "v_pk_fma_f32 v[156:157], v[156:157], v[142:143], v[144:145] op_sel_hi:[1,0,0] neg_lo:[0,0,1] neg_hi:[0,0,1]\n"
    "v_cvt_pk_bf16_f32 v56, v156, 1.0\n"
    "v_cvt_pk_bf16_f32 v57, v157, 1.0\n"
    "v_lshlrev_b32 v158, 16, v17\n"
    "v_and_b32 v159, 0xffff0000, v17\n"
    "v_pk_fma_f32 v[158:159], v[158:159], v[142:143], v[144:145] op_sel_hi:[1,0,0] neg_lo:[0,0,1] neg_hi:[0,0,1]\n"
    "v_cvt_pk_bf16_f32 v58, v158, 1.0\n"
    "v_cvt_pk_bf16_f32 v59, v159, 1.0\n"
    "v_lshlrev_b32 v156, 16, v18\n"
    "v_and_b32 v157, 0xffff0000, v18\n"
    "v_pk_fma_f32 v[156:157], v[156:157], v[142:143], v[144:145] op_sel_hi:[1,0,0] neg_lo:[0,0,1] neg_hi:[0,0,1]\n"
    "v_cvt_pk_bf16_f32 v60, v156, 1.0\n"
    "v_cvt_pk_bf16_f32 v61, v157, 1.0\n"
    "v_lshlrev_b32 v158, 16, v19\n"
    "v_and_b32 v159, 0xffff0000, v19\n"
    "v_pk_fma_f32 v[158:159], v[158:159], v[142:143], v[144:145] op_sel_hi:[1,0,0] neg_lo:[0,0,1] neg_hi:[0,0,1]\n"
    "v_cvt_pk_bf16_f32 v62, v158, 1.0\n"
    "v_cvt_pk_bf16_f32 v63, v159, 1.0\n"
    "v_lshlrev_b32 v156, 16, v20\n"
    "v_and_b32 v157, 0xffff0000, v20\n"
    "v_pk_fma_f32 v[156:157], v[156:157], v[142:143], v[144:145] op_sel_hi:[1,0,0] neg_lo:[0,0,1] neg_hi:[0,0,1]\n"
    "v_cvt_pk_bf16_f32 v64, v156, 1.0\n"
    "v_cvt_pk_bf16_f32 v65, v157, 1.0\n"
    "v_lshlrev_b32 v158, 16, v21\n"
    "v_and_b32 v159, 0xffff0000, v21\n"
    "v_pk_fma_f32 v[158:159], v[158:159], v[142:143], v[144:145] op_sel_hi:[1,0,0] neg_lo:[0,0,1] neg_hi:[0,0,1]\n"
    "v_cvt_pk_bf16_f32 v66, v158, 1.0\n"
    "v_cvt_pk_bf16_f32 v67, v159, 1.0\n"
    "v_lshlrev_b32 v156, 16, v22\n"
    "v_and_b32 v157, 0xffff0000, v22\n"
    "v_pk_fma_f32 v[156:157], v[156:157], v[142:143], v[144:145] op_sel_hi:[1,0,0] neg_lo:[0,0,1] neg_hi:[0,0,1]\n"
    "v_cvt_pk_bf16_f32 v68, v156, 1.0\n"
    "v_cvt_pk_bf16_f32 v69, v157, 1.0\n"
    "v_lshlrev_b32 v158, 16, v23\n"
    "v_and_b32 v159, 0xffff0000, v23\n"
    "v_pk_fma_f32 v[158:159], v[158:159], v[142:143], v[144:145] op_sel_hi:[1,0,0] neg_lo:[0,0,1] neg_hi:[0,0,1]\n"
    "v_cvt_pk_bf16_f32 v70, v158, 1.0\n"
    "v_cvt_pk_bf16_f32 v71, v159, 1.0\n"
    "v_lshlrev_b32 v156, 16, v24\n"
    "v_and_b32 v157, 0xffff0000, v24\n"
    "v_pk_fma_f32 v[156:157], v[156:157], v[142:143], v[144:145] op_sel_hi:[1,0,0] neg_lo:[0,0,1] neg_hi:[0,0,1]\n"
    "v_cvt_pk_bf16_f32 v72, v156, 1.0\n"
    "v_cvt_pk_bf16_f32 v73, v157, 1.0\n"
    "v_lshlrev_b32 v158, 16, v25\n"
    "v_and_b32 v159, 0xffff0000, v25\n"
    "v_pk_fma_f32 v[158:159], v[158:159], v[142:143], v[144:145] op_sel_hi:[1,0,0] neg_lo:[0,0,1] neg_hi:[0,0,1]\n"
    "v_cvt_pk_bf16_f32 v74, v158, 1.0\n"
    "v_cvt_pk_bf16_f32 v75, v159, 1.0\n"
    "v_lshlrev_b32 v156, 16, v26\n"
    "v_and_b32 v157, 0xffff0000, v26\n"
    "v_pk_fma_f32 v[156:157], v[156:157], v[142:143], v[144:145] op_sel_hi:[1,0,0] neg_lo:[0,0,1] neg_hi:[0,0,1]\n"
    "v_cvt_pk_bf16_f32 v76, v156, 1.0\n"
    "v_cvt_pk_bf16_f32 v77, v157, 1.0\n"
    "v_lshlrev_b32 v158, 16, v27\n"
    "v_and_b32 v159, 0xffff0000, v27\n"
    "v_pk_fma_f32 v[158:159], v[158:159], v[142:143], v[144:145] op_sel_hi:[1,0,0] neg_lo:[0,0,1] neg_hi:[0,0,1]\n"
    "v_cvt_pk_bf16_f32 v78, v158, 1.0\n"
    "v_cvt_pk_bf16_f32 v79, v159, 1.0\n"
    "v_lshlrev_b32 v156, 16, v28\n"
    "v_and_b32 v157, 0xffff0000, v28\n"
    "v_pk_fma_f32 v[156:157], v[156:157], v[142:143], v[144:145] op_sel_hi:[1,0,0] neg_lo:[0,0,1] neg_hi:[0,0,1]\n"
    "v_cvt_pk_bf16_f32 v80, v156, 1.0\n"
    "v_cvt_pk_bf16_f32 v81, v157, 1.0\n"
    "v_lshlrev_b32 v158, 16, v29\n"
    "v_and_b32 v159, 0xffff0000, v29\n"
    "v_pk_fma_f32 v[158:159], v[158:159], v[142:143], v[144:145] op_sel_hi:[1,0,0] neg_lo:[0,0,1] neg_hi:[0,0,1]\n"
    "v_cvt_pk_bf16_f32 v82, v158, 1.0\n"
    "v_cvt_pk_bf16_f32 v83, v159, 1.0\n"
    "v_lshlrev_b32 v156, 16, v30\n"
    "v_and_b32 v157, 0xffff0000, v30\n"
    "v_pk_fma_f32 v[156:157], v[156:157], v[142:143], v[144:145] op_sel_hi:[1,0,0] neg_lo:[0,0,1] neg_hi:[0,0,1]\n"
    "v_cvt_pk_bf16_f32 v84, v156, 1.0\n"
    "v_cvt_pk_bf16_f32 v85, v157, 1.0\n"
    "v_lshlrev_b32 v158, 16, v31\n"
    "v_and_b32 v159, 0xffff0000, v31\n"
    "v_pk_fma_f32 v[158:159], v[158:159], v[142:143], v[144:145] op_sel_hi:[1,0,0] neg_lo:[0,0,1] neg_hi:[0,0,1]\n"
    "v_cvt_pk_bf16_f32 v86, v158, 1.0\n"
    "v_cvt_pk_bf16_f32 v87, v159, 1.0\n"
    "s_waitcnt vmcnt(0)\n"
    "v_readfirstlane_b32 s44, v166\n"
    "s_mul_i32 s44, s44, 1664525\n"
    "s_add_u32 s44, s44, 1013904223\n"
    "s_not_b32 s45, s44\n"
    "v_mov_b32 v2, s44\n"
    "v_mov_b32 v3, s45\n"
    "s_barrier\n"
    "s_sub_u32 s59, s3, 8\n"
    "s_cmp_ge_u32 s59, 3\n"
    "s_cbranch_scc1 L_nomod\n"
    "s_mul_i32 s56, s4, 6144\n"
    "s_lshr_b32 s58, s4, 2\n"
    "s_mul_i32 s58, s58, 3072\n"
    "s_add_u32 s56, s56, s58\n"
    "s_lshl_b32 s57, s59, 9\n"
    "s_add_u32 s56, s56, s57\n"
    "s_add_u32 s59, s56, 274432\n"
    "v_lshlrev_b32 v154, 3, v1\n"
    "buffer_load_dwordx2 v[156:157], v154, s[40:43], s59 offen sc1\n"
    "buffer_load_dwordx2 v[158:159], v154, s[40:43], s59 offen offset:1536 sc1\n"
    "s_add_u32 s57, s57, 161792\n"
    "v_add_u32 v155, s57, v154\n"
    "s_and_b32 s56, s3, 1\n"
    "s_bfe_u32 s57, s3, 0x10001\n"
    "s_lshl_b32 s58, s6, 1\n"
    "s_or_b32 s58, s58, s57\n"
    "s_lshl_b32 s0, s7, 1\n"
    "s_or_b32 s0, s0, s56\n"
    "s_lshl_b32 s1, s4, 4\n"
    "s_cmp_ge_u32 s3, 8\n"
    "s_cbranch_scc1 L_p1v_2\n"
    "s_or_b32 s1, s1, s0\n"
    "s_lshl_b32 s0, s1, 3\n"
    "s_or_b32 s0, s0, s58\n"
    "s_lshl_b32 s52, s0, 11\n"
    "s_lshl_b32 s0, s1, 4\n"
    "s_or_b32 s0, s0, s58\n"
    "s_lshl_b32 s53, s0, 3\n"
    "s_lshr_b32 s0, s3, 2\n"
    "s_lshl_b32 s1, s0, 21\n"
    "s_sub_u32 s52, s52, s1\n"
    "s_add_u32 s52, s52, 3145728\n"
    "s_lshl_b32 s1, s0, 14\n"
    "s_sub_u32 s53, s53, s1\n"
    "s_add_u32 s53, s53, 32768\n"
    "s_branch L_p1e_3\n"
    "L_p1v_2:\n"
    "s_lshl_b32 s52, s58, 1\n"
    "s_or_b32 s52, s52, s1\n"
    "s_lshl_b32 s52, s52, 4\n"
    "s_or_b32 s52, s52, s0\n"
    "s_lshl_b32 s52, s52, 10\n"
    "s_add_u32 s52, s52, 5242880\n"
    "s_and_b32 s53, s0, 8\n"
    "s_or_b32 s53, s53, s1\n"
    "s_or_b32 s53, s53, s58\n"
    "s_lshl_b32 s53, s53, 4\n"
    "s_and_b32 s0, s0, 7\n"
    "s_or_b32 s53, s53, s0\n"
    "s_lshl_b32 s53, s53, 3\n"
    "s_add_u32 s53, s53, 49152\n"
    "L_p1e_3:\n"
    "v_bfe_u32 v5, v1, 2, 2\n"
    "v_lshrrev_b32 v6, 1, v5\n"
    "v_xor_b32 v6, s57, v6\n"
    "v_lshlrev_b32 v5, 7, v5\n"
    "v_lshl_add_u32 v5, v6, 6, v5\n"
    "v_bfe_u32 v6, v1, 4, 1\n"
    "v_lshl_add_u32 v5, v6, 5, v5\n"
    "v_and_b32 v6, 3, v1\n"
    "v_lshl_add_u32 v5, v6, 3, v5\n"
    "v_lshrrev_b32 v6, 5, v1\n"
    "v_lshl_add_u32 v5, v6, 10, v5\n"
    "s_lshr_b32 s0, s3, 2\n"
    "s_mul_i32 s0, s0, 49152\n"
    "v_add_u32 v5, s0, v5\n"
    "s_mul_i32 s56, s56, 24576\n"
    "v_xor_b32 v7, v1, v6\n"
    "v_xor_b32 v150, 0, v7\n"
    "v_lshl_add_u32 v150, v150, 4, s56\n"
    "v_xor_b32 v151, 2, v7\n"
    "v_lshl_add_u32 v151, v151, 4, s56\n"
    "v_xor_b32 v152, 4, v7\n"
    "v_lshl_add_u32 v152, v152, 4, s56\n"
    "v_xor_b32 v153, 6, v7\n"
    "v_lshl_add_u32 v153, v153, 4, s56\n"
    "s_movk_i32 s46, 32767\n"
    "s_mov_b32 s57, 0x05040100\n"
    "s_mov_b32 s58, 0x07060302\n"
    "L_modpoll:\n"
    "s_waitcnt vmcnt(1)\n"
    "v_cmp_ne_u32 s[0:1], s44, v157\n"
    "s_waitcnt vmcnt(0)\n"
    "v_cmp_ne_u32 vcc, s44, v159\n"
    "s_or_b64 vcc, vcc, s[0:1]\n"
    "s_cbranch_scc0 L_modok\n"
    "buffer_load_dwordx2 v[156:157], v154, s[40:43], s59 offen sc1\n"
    "buffer_load_dwordx2 v[158:159], v154, s[40:43], s59 offen offset:1536 sc1\n"
    "s_sub_u32 s46, s46, 1\n"
    "s_cbranch_scc0 L_modpoll\n"
    "L_modok:\n"
    "s_waitcnt vmcnt(0)\n"
    "v_perm_b32 v160, v156, v158, s57\n"
    "v_perm_b32 v161, v156, v158, s58\n"
    "ds_write_b64 v155, v[160:161]\n"
    "s_branch L_modend1\n"
    "L_nomod:\n"
    "s_and_b32 s56, s3, 1\n"
    "s_bfe_u32 s57, s3, 0x10001\n"
    "s_lshl_b32 s58, s6, 1\n"
    "s_or_b32 s58, s58, s57\n"
    "s_lshl_b32 s0, s7, 1\n"
    "s_or_b32 s0, s0, s56\n"
    "s_lshl_b32 s1, s4, 4\n"
    "s_cmp_ge_u32 s3, 8\n"
    "s_cbranch_scc1 L_p1v_4\n"
    "s_or_b32 s1, s1, s0\n"
    "s_lshl_b32 s0, s1, 3\n"
    "s_or_b32 s0, s0, s58\n"
    "s_lshl_b32 s52, s0, 11\n"
    "s_lshl_b32 s0, s1, 4\n"
    "s_or_b32 s0, s0, s58\n"
    "s_lshl_b32 s53, s0, 3\n"
    "s_lshr_b32 s0, s3, 2\n"
    "s_lshl_b32 s1, s0, 21\n"
    "s_sub_u32 s52, s52, s1\n"
    "s_add_u32 s52, s52, 3145728\n"
    "s_lshl_b32 s1, s0, 14\n"
    "s_sub_u32 s53, s53, s1\n"
    "s_add_u32 s53, s53, 32768\n"
    "s_branch L_p1e_5\n"
    "L_p1v_4:\n"
    "s_lshl_b32 s52, s58, 1\n"
    "s_or_b32 s52, s52, s1\n"
    "s_lshl_b32 s52, s52, 4\n"
    "s_or_b32 s52, s52, s0\n"
    "s_lshl_b32 s52, s52, 10\n"
    "s_add_u32 s52, s52, 5242880\n"
    "s_and_b32 s53, s0, 8\n"
    "s_or_b32 s53, s53, s1\n"
    "s_or_b32 s53, s53, s58\n"
    "s_lshl_b32 s53, s53, 4\n"
    "s_and_b32 s0, s0, 7\n"
    "s_or_b32 s53, s53, s0\n"
    "s_lshl_b32 s53, s53, 3\n"
    "s_add_u32 s53, s53, 49152\n"
    "L_p1e_5:\n"
    "v_bfe_u32 v5, v1, 2, 2\n"
    "v_lshrrev_b32 v6, 1, v5\n"
    "v_xor_b32 v6, s57, v6\n"
    "v_lshlrev_b32 v5, 7, v5\n"
    "v_lshl_add_u32 v5, v6, 6, v5\n"
    "v_bfe_u32 v6, v1, 4, 1\n"
    "v_lshl_add_u32 v5, v6, 5, v5\n"
    "v_and_b32 v6, 3, v1\n"
    "v_lshl_add_u32 v5, v6, 3, v5\n"
    "v_lshrrev_b32 v6, 5, v1\n"
    "v_lshl_add_u32 v5, v6, 10, v5\n"
    "s_lshr_b32 s0, s3, 2\n"
    "s_mul_i32 s0, s0, 49152\n"
    "v_add_u32 v5, s0, v5\n"
    "s_mul_i32 s56, s56, 24576\n"
    "v_xor_b32 v7, v1, v6\n"
    "v_xor_b32 v150, 0, v7\n"
    "v_lshl_add_u32 v150, v150, 4, s56\n"
    "v_xor_b32 v151, 2, v7\n"
    "v_lshl_add_u32 v151, v151, 4, s56\n"
    "v_xor_b32 v152, 4, v7\n"
    "v_lshl_add_u32 v152, v152, 4, s56\n"
    "v_xor_b32 v153, 6, v7\n"
    "v_lshl_add_u32 v153, v153, 4, s56\n"
    "L_modend1:\n"
    "s_waitcnt lgkmcnt(0)\n"
    "s_barrier\n"
    "ds_read_b128 v[8:11], v149 offset:0\n"
    "ds_read_b128 v[12:15], v149 offset:16\n"
    "ds_read_b128 v[16:19], v149 offset:256\n"
    "ds_read_b128 v[20:23], v149 offset:272\n"
    "ds_read_b128 v[24:27], v149 offset:512\n"
    "ds_read_b128 v[28:31], v149 offset:528\n"
    "ds_read_b128 v[88:91], v149 offset:768\n"
    "ds_read_b128 v[92:95], v149 offset:784\n"
    "ds_read_b128 v[96:99], v149 offset:1024\n"
    "ds_read_b128 v[100:103], v149 offset:1040\n"
    "ds_read_b128 v[104:107], v149 offset:1280\n"
    "ds_read_b128 v[108:111], v149 offset:1296\n"
    "s_waitcnt lgkmcnt(11)\n"
    "v_dot2_f32_bf16 v40, v8, v40, 0\n"
    "v_dot2_f32_bf16 v41, v9, v41, 0\n"
    "v_dot2_f32_bf16 v42, v10, v42, 0\n"
    "v_dot2_f32_bf16 v43, v11, v43, 0\n"
    "s_waitcnt lgkmcnt(10)\n"
    "v_dot2_f32_bf16 v44, v12, v44, 0\n"
    "v_dot2_f32_bf16 v45, v13, v45, 0\n"
    "v_dot2_f32_bf16 v46, v14, v46, 0\n"
    "v_dot2_f32_bf16 v47, v15, v47, 0\n"
    "s_waitcnt lgkmcnt(9)\n"
    "v_dot2_f32_bf16 v48, v16, v48, 0\n"
    "v_dot2_f32_bf16 v49, v17, v49, 0\n"
    "v_dot2_f32_bf16 v50, v18, v50, 0\n"
    "v_dot2_f32_bf16 v51, v19, v51, 0\n"
    "s_waitcnt lgkmcnt(8)\n"
    "v_dot2_f32_bf16 v52, v20, v52, 0\n"
    "v_dot2_f32_bf16 v53, v21, v53, 0\n"
    "v_dot2_f32_bf16 v54, v22, v54, 0\n"
    "v_dot2_f32_bf16 v55, v23, v55, 0\n"
    "v_cvt_pk_bf16_f32 v40, v40, v41\n"
    "v_cvt_pk_bf16_f32 v41, v42, v43\n"
    "v_cvt_pk_bf16_f32 v42, v44, v45\n"
    "v_cvt_pk_bf16_f32 v43, v46, v47\n"
    "ds_write_b128 v148, v[40:43] offset:0\n"
    "s_waitcnt lgkmcnt(8)\n"
    "v_dot2_f32_bf16 v56, v24, v56, 0\n"
    "v_dot2_f32_bf16 v57, v25, v57, 0\n"
    "v_dot2_f32_bf16 v58, v26, v58, 0\n"
    "v_dot2_f32_bf16 v59, v27, v59, 0\n"
    "s_waitcnt lgkmcnt(7)\n"
    "v_dot2_f32_bf16 v60, v28, v60, 0\n"
    "v_dot2_f32_bf16 v61, v29, v61, 0\n"
    "v_dot2_f32_bf16 v62, v30, v62, 0\n"
    "v_dot2_f32_bf16 v63, v31, v63, 0\n"
    "v_cvt_pk_bf16_f32 v48, v48, v49\n"
    "v_cvt_pk_bf16_f32 v49, v50, v51\n"
    "v_cvt_pk_bf16_f32 v50, v52, v53\n"
    "v_cvt_pk_bf16_f32 v51, v54, v55\n"
    "ds_write_b128 v148, v[48:51] offset:4096\n"
    "s_waitcnt lgkmcnt(7)\n"
    "v_dot2_f32_bf16 v64, v88, v64, 0\n"
    "v_dot2_f32_bf16 v65, v89, v65, 0\n"
    "v_dot2_f32_bf16 v66, v90, v66, 0\n"
    "v_dot2_f32_bf16 v67, v91, v67, 0\n"
    "s_waitcnt lgkmcnt(6)\n"
    "v_dot2_f32_bf16 v68, v92, v68, 0\n"
    "v_dot2_f32_bf16 v69, v93, v69, 0\n"
    "v_dot2_f32_bf16 v70, v94, v70, 0\n"
    "v_dot2_f32_bf16 v71, v95, v71, 0\n"
    "v_cvt_pk_bf16_f32 v56, v56, v57\n"
    "v_cvt_pk_bf16_f32 v57, v58, v59\n"
    "v_cvt_pk_bf16_f32 v58, v60, v61\n"
    "v_cvt_pk_bf16_f32 v59, v62, v63\n"
    "ds_write_b128 v148, v[56:59] offset:8192\n"
    "s_waitcnt lgkmcnt(6)\n"
    "v_dot2_f32_bf16 v72, v96, v72, 0\n"
    "v_dot2_f32_bf16 v73, v97, v73, 0\n"
    "v_dot2_f32_bf16 v74, v98, v74, 0\n"
    "v_dot2_f32_bf16 v75, v99, v75, 0\n"
    "s_waitcnt lgkmcnt(5)\n"
    "v_dot2_f32_bf16 v76, v100, v76, 0\n"
    "v_dot2_f32_bf16 v77, v101, v77, 0\n"
    "v_dot2_f32_bf16 v78, v102, v78, 0\n"
    "v_dot2_f32_bf16 v79, v103, v79, 0\n"
    "v_cvt_pk_bf16_f32 v64, v64, v65\n"
    "v_cvt_pk_bf16_f32 v65, v66, v67\n"
    "v_cvt_pk_bf16_f32 v66, v68, v69\n"
    "v_cvt_pk_bf16_f32 v67, v70, v71\n"
    "ds_write_b128 v148, v[64:67] offset:12288\n"
    "s_waitcnt lgkmcnt(5)\n"
    "v_dot2_f32_bf16 v80, v104, v80, 0\n"
    "v_dot2_f32_bf16 v81, v105, v81, 0\n"
    "v_dot2_f32_bf16 v82, v106, v82, 0\n"
    "v_dot2_f32_bf16 v83, v107, v83, 0\n"
    "s_waitcnt lgkmcnt(4)\n"
    "v_dot2_f32_bf16 v84, v108, v84, 0\n"
    "v_dot2_f32_bf16 v85, v109, v85, 0\n"
    "v_dot2_f32_bf16 v86, v110, v86, 0\n"
    "v_dot2_f32_bf16 v87, v111, v87, 0\n"
    "v_cvt_pk_bf16_f32 v72, v72, v73\n"
    "v_cvt_pk_bf16_f32 v73, v74, v75\n"
    "v_cvt_pk_bf16_f32 v74, v76, v77\n"
    "v_cvt_pk_bf16_f32 v75, v78, v79\n"
    "ds_write_b128 v148, v[72:75] offset:16384\n"
    "v_cvt_pk_bf16_f32 v80, v80, v81\n"
    "v_cvt_pk_bf16_f32 v81, v82, v83\n"
    "v_cvt_pk_bf16_f32 v82, v84, v85\n"
    "v_cvt_pk_bf16_f32 v83, v86, v87\n"
    "ds_write_b128 v148, v[80:83] offset:20480\n"
    "s_waitcnt lgkmcnt(0)\n"
    "s_barrier\n"
    "s_cmp_ge_u32 s3, 8\n"
    "s_cbranch_scc1 L_vw\n"
    "ds_read_b64_tr_b16 v[8:9], v5 offset:0\n"
    "ds_read_b64_tr_b16 v[10:11], v5 offset:512\n"
    "ds_read_b64_tr_b16 v[12:13], v5 offset:2048\n"
    "ds_read_b64_tr_b16 v[14:15], v5 offset:2560\n"
    "ds_read_b64_tr_b16 v[16:17], v5 offset:4096\n"
    "ds_read_b64_tr_b16 v[18:19], v5 offset:4608\n"
    "ds_read_b64_tr_b16 v[20:21], v5 offset:6144\n"
    "ds_read_b64_tr_b16 v[22:23], v5 offset:6656\n"
    "ds_read_b64_tr_b16 v[24:25], v5 offset:8192\n"
    "ds_read_b64_tr_b16 v[26:27], v5 offset:8704\n"
    "ds_read_b64_tr_b16 v[28:29], v5 offset:10240\n"
    "ds_read_b64_tr_b16 v[30:31], v5 offset:10752\n"
    "ds_read_b64_tr_b16 v[32:33], v5 offset:12288\n"
    "ds_read_b64_tr_b16 v[34:35], v5 offset:12800\n"
    "ds_read_b64_tr_b16 v[36:37], v5 offset:14336\n"
    "ds_read_b64_tr_b16 v[38:39], v5 offset:14848\n"
    "ds_read_b64_tr_b16 v[40:41], v5 offset:16384\n"
    "ds_read_b64_tr_b16 v[42:43], v5 offset:16896\n"
    "ds_read_b64_tr_b16 v[44:45], v5 offset:18432\n"
    "ds_read_b64_tr_b16 v[46:47], v5 offset:18944\n"
    "ds_read_b64_tr_b16 v[48:49], v5 offset:20480\n"
    "ds_read_b64_tr_b16 v[50:51], v5 offset:20992\n"
    "ds_read_b64_tr_b16 v[52:53], v5 offset:22528\n"
    "ds_read_b64_tr_b16 v[54:55], v5 offset:23040\n"
    "ds_read_b64_tr_b16 v[56:57], v5 offset:24576\n"
    "ds_read_b64_tr_b16 v[58:59], v5 offset:25088\n"
    "ds_read_b64_tr_b16 v[60:61], v5 offset:26624\n"
    "ds_read_b64_tr_b16 v[62:63], v5 offset:27136\n"
    "ds_read_b64_tr_b16 v[64:65], v5 offset:28672\n"
    "ds_read_b64_tr_b16 v[66:67], v5 offset:29184\n"
    "ds_read_b64_tr_b16 v[68:69], v5 offset:30720\n"
    "ds_read_b64_tr_b16 v[70:71], v5 offset:31232\n"
    "ds_read_b64_tr_b16 v[72:73], v5 offset:32768\n"
    "ds_read_b64_tr_b16 v[74:75], v5 offset:33280\n"
    "ds_read_b64_tr_b16 v[76:77], v5 offset:34816\n"
    "ds_read_b64_tr_b16 v[78:79], v5 offset:35328\n"
    "ds_read_b64_tr_b16 v[80:81], v5 offset:36864\n"
    "ds_read_b64_tr_b16 v[82:83], v5 offset:37376\n"
    "ds_read_b64_tr_b16 v[84:85], v5 offset:38912\n"
    "ds_read_b64_tr_b16 v[86:87], v5 offset:39424\n"
    "ds_read_b64_tr_b16 v[88:89], v5 offset:40960\n"
    "ds_read_b64_tr_b16 v[90:91], v5 offset:41472\n"
    "ds_read_b64_tr_b16 v[92:93], v5 offset:43008\n"
    "ds_read_b64_tr_b16 v[94:95], v5 offset:43520\n"
    "ds_read_b64_tr_b16 v[96:97], v5 offset:45056\n"
    "ds_read_b64_tr_b16 v[98:99], v5 offset:45568\n"
    "ds_read_b64_tr_b16 v[100:101], v5 offset:47104\n"
    "ds_read_b64_tr_b16 v[102:103], v5 offset:47616\n"
    "s_branch L_gemm1\n"
    "L_kw1:\n"
    "s_movk_i32 s47, 4096\n"
    "s_movk_i32 s48, 8192\n"
    "s_movk_i32 s49, 12288\n"
    "s_movk_i32 s50, 16384\n"
    "s_movk_i32 s51, 20480\n"
    "v_lshlrev_b32 v0, 4, v1\n"
    "s_mul_i32 s56, s3, 24576\n"
    "s_add_u32 s56, s56, 24117248\n"
    "v_add_u32 v4, s56, v0\n"
    "s_and_b32 s56, s5, 1\n"
    "s_lshl_b32 s57, s4, 4\n"
    "s_lshl_b32 s58, s56, 3\n"
    "s_or_b32 s57, s57, s58\n"
    "s_lshr_b32 s58, s3, 1\n"
    "s_or_b32 s58, s58, s57\n"
    "s_lshl_b32 s58, s58, 7\n"
    "s_add_u32 s60, s58, 49152\n"
    "s_add_u32 s57, s57, s3\n"
    "s_and_b32 s58, s4, 5\n"
    "s_lshl_b32 s58, s58, 4\n"
    "s_and_b32 s0, s5, 16\n"
    "s_lshl_b32 s0, s0, 1\n"
    "s_or_b32 s58, s58, s0\n"
    "s_and_b32 s0, s5, 14\n"
    "s_or_b32 s58, s58, s0\n"
    "s_bfe_u32 s0, s3, 0x10001\n"
    "s_or_b32 s58, s58, s0\n"
    "s_and_b32 s0, s3, 1\n"
    "s_mul_i32 s1, s0, 6144\n"
    "s_add_u32 s1, s1, 3145728\n"
    "s_mul_i32 s0, s0, 24\n"
    "s_add_u32 s0, s0, 32768\n"
    "s_lshl_b32 s34, s58, 14\n"
    "s_add_u32 s34, s34, s1\n"
    "s_lshl_b32 s35, s58, 7\n"
    "s_add_u32 s35, s35, s0\n"
    "s_lshl_b32 s58, s57, 14\n"
    "s_add_u32 s54, s58, 1048576\n"
    "s_lshl_b32 s58, s57, 7\n"
    "s_add_u32 s55, s58, 16384\n"
    "s_and_b32 s57, s4, 4\n"
    "s_lshl_b32 s57, s57, 2\n"
    "s_lshr_b32 s58, s5, 1\n"
    "s_or_b32 s57, s57, s58\n"
    "s_and_b32 s58, s4, 1\n"
    "s_lshl_b32 s58, s58, 2\n"
    "s_and_b32 s0, s4, 2\n"
    "s_or_b32 s58, s58, s0\n"
    "s_or_b32 s58, s58, s56\n"
    "s_lshl_b32 s0, s57, 4\n"
    "s_or_b32 s1, s0, s58\n"
    "s_lshl_b32 s1, s1, 4\n"
    "s_or_b32 s1, s1, s3\n"
    "s_lshl_b32 s1, s1, 10\n"
    "s_add_u32 s61, s1, 7340032\n"
    "s_or_b32 s0, s0, s3\n"
    "s_lshl_b32 s0, s0, 4\n"
    "s_or_b32 s0, s0, s58\n"
    "s_lshl_b32 s0, s0, 3\n"
    "s_add_u32 s62, s0, 65536\n"
    "s_lshl_b32 s57, s4, 3\n"
    "s_or_b32 s57, s57, s7\n"
    "s_mul_i32 s58, s57, 24576\n"
    "s_lshl_b32 s0, s3, 6\n"
    "s_add_u32 s63, s58, s0\n"
    "s_lshr_b32 s58, s4, 2\n"
    "s_mul_i32 s58, s58, 6144\n"
    "s_lshl_b32 s0, s3, 9\n"
    "s_add_u32 s58, s58, s0\n"
    "s_add_u32 s96, s58, 323584\n"
    "s_lshl_b32 s58, s57, 7\n"
    "s_or_b32 s58, s58, s3\n"
    "s_lshl_b32 s58, s58, 10\n"
    "s_add_u32 s97, s58, 7340032\n"
    "s_lshr_b32 s58, s57, 1\n"
    "s_lshl_b32 s0, s58, 4\n"
    "s_or_b32 s0, s0, s3\n"
    "s_lshl_b32 s0, s0, 7\n"
    "s_add_u32 s98, s0, 65536\n"
    "s_lshl_b32 s58, s58, 4\n"
    "s_and_b32 s0, s57, 1\n"
    "s_or_b32 s58, s58, s0\n"
    "s_lshl_b32 s58, s58, 8\n"
    "s_add_u32 s99, s58, 393216\n"
    "s_and_b32 s56, s3, 1\n"
    "s_bfe_u32 s57, s3, 0x10001\n"
    "s_lshl_b32 s58, s6, 1\n"
    "s_or_b32 s58, s58, s57\n"
    "s_lshl_b32 s0, s7, 1\n"
    "s_or_b32 s0, s0, s56\n"
    "s_lshl_b32 s1, s4, 4\n"
    "s_cmp_ge_u32 s3, 8\n"
    "s_cbranch_scc1 L_p1v_6\n"
    "s_or_b32 s1, s1, s0\n"
    "s_lshl_b32 s0, s1, 3\n"
    "s_or_b32 s0, s0, s58\n"
    "s_lshl_b32 s52, s0, 11\n"
    "s_lshl_b32 s0, s1, 4\n"
    "s_or_b32 s0, s0, s58\n"
    "s_lshl_b32 s53, s0, 3\n"
    "s_lshr_b32 s0, s3, 2\n"
    "s_lshl_b32 s1, s0, 21\n"
    "s_sub_u32 s52, s52, s1\n"
    "s_add_u32 s52, s52, 3145728\n"
    "s_lshl_b32 s1, s0, 14\n"
    "s_sub_u32 s53, s53, s1\n"
    "s_add_u32 s53, s53, 32768\n"
    "s_branch L_p1e_7\n"
    "L_p1v_6:\n"
    "s_lshl_b32 s52, s58, 1\n"
    "s_or_b32 s52, s52, s1\n"
    "s_lshl_b32 s52, s52, 4\n"
    "s_or_b32 s52, s52, s0\n"
    "s_lshl_b32 s52, s52, 10\n"
    "s_add_u32 s52, s52, 5242880\n"
    "s_and_b32 s53, s0, 8\n"
    "s_or_b32 s53, s53, s1\n"
    "s_or_b32 s53, s53, s58\n"
    "s_lshl_b32 s53, s53, 4\n"
    "s_and_b32 s0, s0, 7\n"
    "s_or_b32 s53, s53, s0\n"
    "s_lshl_b32 s53, s53, 3\n"
    "s_add_u32 s53, s53, 49152\n"
    "L_p1e_7:\n"
    "v_bfe_u32 v5, v1, 2, 2\n"
    "v_lshrrev_b32 v6, 1, v5\n"
    "v_xor_b32 v6, s57, v6\n"
    "v_lshlrev_b32 v5, 7, v5\n"
    "v_lshl_add_u32 v5, v6, 6, v5\n"
    "v_bfe_u32 v6, v1, 4, 1\n"
    "v_lshl_add_u32 v5, v6, 5, v5\n"
    "v_and_b32 v6, 3, v1\n"
    "v_lshl_add_u32 v5, v6, 3, v5\n"
    "v_lshrrev_b32 v6, 5, v1\n"
    "v_lshl_add_u32 v5, v6, 10, v5\n"
    "s_lshr_b32 s0, s3, 2\n"
    "s_mul_i32 s0, s0, 49152\n"
    "v_add_u32 v5, s0, v5\n"
    "s_mul_i32 s56, s56, 24576\n"
    "v_xor_b32 v7, v1, v6\n"
    "v_xor_b32 v150, 0, v7\n"
    "v_lshl_add_u32 v150, v150, 4, s56\n"
    "v_xor_b32 v151, 2, v7\n"
    "v_lshl_add_u32 v151, v151, 4, s56\n"
    "v_xor_b32 v152, 4, v7\n"
    "v_lshl_add_u32 v152, v152, 4, s56\n"
    "v_xor_b32 v153, 6, v7\n"
    "v_lshl_add_u32 v153, v153, 4, s56\n"
    "s_add_u32 m0, s28, 49152\n"
    "s_add_u32 s24, s24, 768\n"
    "buffer_load_dwordx4 v163, s[16:19], s24 offen offset:0 lds\n"
    "buffer_load_dwordx4 v164, s[16:19], s24 offen offset:1024 lds\n"
    "buffer_load_dwordx4 v165, s[16:19], s24 offen offset:2048 lds\n"
    "s_add_u32 m0, s29, 49152\n"
    "s_add_u32 s25, s25, 768\n"
    "buffer_load_dwordx4 v163, s[16:19], s25 offen offset:0 lds\n"
    "buffer_load_dwordx4 v164, s[16:19], s25 offen offset:1024 lds\n"
    "buffer_load_dwordx4 v165, s[16:19], s25 offen offset:2048 lds\n"
    "s_add_u32 m0, s30, 49152\n"
    "s_add_u32 s26, s26, 768\n"
    "buffer_load_dwordx4 v163, s[16:19], s26 offen offset:0 lds\n"
    "buffer_load_dwordx4 v164, s[16:19], s26 offen offset:1024 lds\n"
    "buffer_load_dwordx4 v165, s[16:19], s26 offen offset:2048 lds\n"
    "s_add_u32 m0, s31, 49152\n"
    "s_add_u32 s27, s27, 768\n"
    "buffer_load_dwordx4 v163, s[16:19], s27 offen offset:0 lds\n"
    "buffer_load_dwordx4 v164, s[16:19], s27 offen offset:1024 lds\n"
    "buffer_load_dwordx4 v165, s[16:19], s27 offen offset:2048 lds\n"
    "buffer_load_dword v166, off, s[40:43], 0 sc1\n"
    "s_waitcnt vmcnt(13)\n"
    "s_barrier\n"
    "ds_read_b64_tr_b16 v[8:9], v5 offset:0\n"
    "ds_read_b64_tr_b16 v[10:11], v5 offset:512\n"
    "ds_read_b64_tr_b16 v[12:13], v5 offset:2048\n"
    "ds_read_b64_tr_b16 v[14:15], v5 offset:2560\n"
    "ds_read_b64_tr_b16 v[16:17], v5 offset:4096\n"
    "ds_read_b64_tr_b16 v[18:19], v5 offset:4608\n"
    "ds_read_b64_tr_b16 v[20:21], v5 offset:6144\n"
    "ds_read_b64_tr_b16 v[22:23], v5 offset:6656\n"
    "ds_read_b64_tr_b16 v[24:25], v5 offset:8192\n"
    "ds_read_b64_tr_b16 v[26:27], v5 offset:8704\n"
    "ds_read_b64_tr_b16 v[28:29], v5 offset:10240\n"
    "ds_read_b64_tr_b16 v[30:31], v5 offset:10752\n"
    "ds_read_b64_tr_b16 v[32:33], v5 offset:12288\n"
    "ds_read_b64_tr_b16 v[34:35], v5 offset:12800\n"
    "ds_read_b64_tr_b16 v[36:37], v5 offset:14336\n"
    "ds_read_b64_tr_b16 v[38:39], v5 offset:14848\n"
    "ds_read_b64_tr_b16 v[40:41], v5 offset:16384\n"
    "ds_read_b64_tr_b16 v[42:43], v5 offset:16896\n"
    "ds_read_b64_tr_b16 v[44:45], v5 offset:18432\n"
    "ds_read_b64_tr_b16 v[46:47], v5 offset:18944\n"
    "ds_read_b64_tr_b16 v[48:49], v5 offset:20480\n"
    "ds_read_b64_tr_b16 v[50:51], v5 offset:20992\n"
    "ds_read_b64_tr_b16 v[52:53], v5 offset:22528\n"
    "ds_read_b64_tr_b16 v[54:55], v5 offset:23040\n"
    "ds_read_b64_tr_b16 v[56:57], v5 offset:24576\n"
    "ds_read_b64_tr_b16 v[58:59], v5 offset:25088\n"
    "ds_read_b64_tr_b16 v[60:61], v5 offset:26624\n"
    "ds_read_b64_tr_b16 v[62:63], v5 offset:27136\n"
    "ds_read_b64_tr_b16 v[64:65], v5 offset:28672\n"
    "ds_read_b64_tr_b16 v[66:67], v5 offset:29184\n"
    "ds_read_b64_tr_b16 v[68:69], v5 offset:30720\n"
    "ds_read_b64_tr_b16 v[70:71], v5 offset:31232\n"
    "ds_read_b64_tr_b16 v[72:73], v5 offset:32768\n"
    "ds_read_b64_tr_b16 v[74:75], v5 offset:33280\n"
    "ds_read_b64_tr_b16 v[76:77], v5 offset:34816\n"
    "ds_read_b64_tr_b16 v[78:79], v5 offset:35328\n"
    "ds_read_b64_tr_b16 v[80:81], v5 offset:36864\n"
    "ds_read_b64_tr_b16 v[82:83], v5 offset:37376\n"
    "ds_read_b64_tr_b16 v[84:85], v5 offset:38912\n"
    "ds_read_b64_tr_b16 v[86:87], v5 offset:39424\n"
    "ds_read_b64_tr_b16 v[88:89], v5 offset:40960\n"
    "ds_read_b64_tr_b16 v[90:91], v5 offset:41472\n"
    "ds_read_b64_tr_b16 v[92:93], v5 offset:43008\n"
    "ds_read_b64_tr_b16 v[94:95], v5 offset:43520\n"
    "ds_read_b64_tr_b16 v[96:97], v5 offset:45056\n"
    "ds_read_b64_tr_b16 v[98:99], v5 offset:45568\n"
    "ds_read_b64_tr_b16 v[100:101], v5 offset:47104\n"
    "ds_read_b64_tr_b16 v[102:103], v5 offset:47616\n"
    "s_waitcnt vmcnt(0)\n"
    "v_readfirstlane_b32 s44, v166\n"
    "s_mul_i32 s44, s44, 1664525\n"
    "s_add_u32 s44, s44, 1013904223\n"
    "s_not_b32 s45, s44\n"
    "v_mov_b32 v2, s44\n"
    "v_mov_b32 v3, s45\n"
    "s_waitcnt lgkmcnt(0)\n"
    "s_barrier\n"
    "s_setprio 2\n"
    "s_barrier\n"
    "L_gemm1:\n"
    "ds_read_b128 v[120:123], v150 offset:0\n"
    "ds_read_b128 v[124:127], v151 offset:1024\n"
    "ds_read_b128 v[128:131], v152 offset:2048\n"
    "ds_read_b128 v[132:135], v153 offset:3072\n"
    "ds_read_b128 v[136:139], v150 offset:4096\n"
    "ds_read_b128 v[140:143], v151 offset:5120\n"
    "s_waitcnt lgkmcnt(5)\n"
    "v_mfma_f32_32x32x16_bf16 v[104:119], v[8:11], v[120:123], 0\n"
    "ds_read_b128 v[120:123], v152 offset:6144\n"
    "s_waitcnt lgkmcnt(5)\n"
    "v_mfma_f32_32x32x16_bf16 v[104:119], v[12:15], v[124:127], v[104:119]\n"
    "ds_read_b128 v[124:127], v153 offset:7168\n"
    "s_waitcnt lgkmcnt(5)\n"
    "v_mfma_f32_32x32x16_bf16 v[104:119], v[16:19], v[128:131], v[104:119]\n"
    "ds_read_b128 v[128:131], v150 offset:8192\n"
    "s_waitcnt lgkmcnt(5)\n"
    "v_mfma_f32_32x32x16_bf16 v[104:119], v[20:23], v[132:135], v[104:119]\n"
    "ds_read_b128 v[132:135], v151 offset:9216\n"
    "s_waitcnt lgkmcnt(5)\n"
    "v_mfma_f32_32x32x16_bf16 v[104:119], v[24:27], v[136:139], v[104:119]\n"
    "ds_read_b128 v[136:139], v152 offset:10240\n"
    "s_waitcnt lgkmcnt(5)\n"
    "v_mfma_f32_32x32x16_bf16 v[104:119], v[28:31], v[140:143], v[104:119]\n"
    "ds_read_b128 v[140:143], v153 offset:11264\n"
    "s_waitcnt lgkmcnt(5)\n"
    "v_mfma_f32_32x32x16_bf16 v[104:119], v[32:35], v[120:123], v[104:119]\n"
    "ds_read_b128 v[120:123], v150 offset:12288\n"
    "s_waitcnt lgkmcnt(5)\n"
    "v_mfma_f32_32x32x16_bf16 v[104:119], v[36:39], v[124:127], v[104:119]\n"
    "ds_read_b128 v[124:127], v151 offset:13312\n"
    "s_waitcnt lgkmcnt(5)\n"
    "v_mfma_f32_32x32x16_bf16 v[104:119], v[40:43], v[128:131], v[104:119]\n"
    "ds_read_b128 v[128:131], v152 offset:14336\n"
    "s_waitcnt lgkmcnt(5)\n"
    "v_mfma_f32_32x32x16_bf16 v[104:119], v[44:47], v[132:135], v[104:119]\n"
    "ds_read_b128 v[132:135], v153 offset:15360\n"
    "s_waitcnt lgkmcnt(5)\n"
    "v_mfma_f32_32x32x16_bf16 v[104:119], v[48:51], v[136:139], v[104:119]\n"
    "ds_read_b128 v[136:139], v150 offset:16384\n"
    "s_waitcnt lgkmcnt(5)\n"
    "v_mfma_f32_32x32x16_bf16 v[104:119], v[52:55], v[140:143], v[104:119]\n"
    "ds_read_b128 v[140:143], v151 offset:17408\n"
    "s_waitcnt lgkmcnt(5)\n"
    "v_mfma_f32_32x32x16_bf16 v[104:119], v[56:59], v[120:123], v[104:119]\n"
    "ds_read_b128 v[120:123], v152 offset:18432\n"
    "s_waitcnt lgkmcnt(5)\n"
    "v_mfma_f32_32x32x16_bf16 v[104:119], v[60:63], v[124:127], v[104:119]\n"
    "ds_read_b128 v[124:127], v153 offset:19456\n"
    "s_waitcnt lgkmcnt(5)\n"
    "v_mfma_f32_32x32x16_bf16 v[104:119], v[64:67], v[128:131], v[104:119]\n"
    "ds_read_b128 v[128:131], v150 offset:20480\n"
    "s_waitcnt lgkmcnt(5)\n"
    "v_mfma_f32_32x32x16_bf16 v[104:119], v[68:71], v[132:135], v[104:119]\n"
    "ds_read_b128 v[132:135], v151 offset:21504\n"
    "s_waitcnt lgkmcnt(5)\n"
    "v_mfma_f32_32x32x16_bf16 v[104:119], v[72:75], v[136:139], v[104:119]\n"
    "ds_read_b128 v[136:139], v152 offset:22528\n"
    "s_waitcnt lgkmcnt(5)\n"
    "v_mfma_f32_32x32x16_bf16 v[104:119], v[76:79], v[140:143], v[104:119]\n"
    "ds_read_b128 v[140:143], v153 offset:23552\n"
    "s_waitcnt lgkmcnt(5)\n"
    "v_mfma_f32_32x32x16_bf16 v[104:119], v[80:83], v[120:123], v[104:119]\n"
    "s_waitcnt lgkmcnt(4)\n"
    "v_mfma_f32_32x32x16_bf16 v[104:119], v[84:87], v[124:127], v[104:119]\n"
    "s_waitcnt lgkmcnt(3)\n"
    "v_mfma_f32_32x32x16_bf16 v[104:119], v[88:91], v[128:131], v[104:119]\n"
    "s_waitcnt lgkmcnt(2)\n"
    "v_mfma_f32_32x32x16_bf16 v[104:119], v[92:95], v[132:135], v[104:119]\n"
    "s_waitcnt lgkmcnt(1)\n"
    "v_mfma_f32_32x32x16_bf16 v[104:119], v[96:99], v[136:139], v[104:119]\n"
    "s_waitcnt lgkmcnt(0)\n"
    "v_mfma_f32_32x32x16_bf16 v[104:119], v[100:103], v[140:143], v[104:119]\n"
    "s_setprio 0\n"
    "s_nop 10\n"
    "v_cvt_pk_bf16_f32 v8, v104, v105\n"
    "v_cvt_pk_bf16_f32 v9, v106, v107\n"
    "v_cvt_pk_bf16_f32 v10, v108, v109\n"
    "v_cvt_pk_bf16_f32 v11, v110, v111\n"
    "buffer_store_dwordx4 v[8:11], v0, s[40:43], s52 offen sc1\n"
    "v_cvt_pk_bf16_f32 v12, v112, v113\n"
    "v_cvt_pk_bf16_f32 v13, v114, v115\n"
    "v_cvt_pk_bf16_f32 v14, v116, v117\n"
    "v_cvt_pk_bf16_f32 v15, v118, v119\n"
    "buffer_store_dwordx4 v[12:15], v0, s[40:43], s52 offen offset:1024 sc1\n"
    "s_waitcnt vmcnt(0)\n"
    "s_mov_b64 exec, 1\n"
    "buffer_store_dwordx2 v[2:3], off, s[40:43], s53 sc1\n"
    "s_mov_b64 exec, -1\n"
    "s_branch L_p2a\n"
    "L_vw:\n"
    "s_add_u32 m0, s25, 0\n"
    "s_nop 0\n"
    "buffer_load_dwordx4 v163, s[16:19], s24 offen offset:0 lds\n"
    "buffer_load_dwordx4 v164, s[16:19], s24 offen offset:1024 lds\n"
    "buffer_load_dwordx4 v165, s[16:19], s24 offen offset:2048 lds\n"
    "s_add_u32 m0, s25, 3072\n"
    "s_add_u32 s24, s24, 55296\n"
    "buffer_load_dwordx4 v163, s[16:19], s24 offen offset:0 lds\n"
    "buffer_load_dwordx4 v164, s[16:19], s24 offen offset:1024 lds\n"
    "buffer_load_dwordx4 v165, s[16:19], s24 offen offset:2048 lds\n"
    "s_add_u32 m0, s25, 6144\n"
    "s_add_u32 s24, s24, 55296\n"
    "buffer_load_dwordx4 v163, s[16:19], s24 offen offset:0 lds\n"
    "buffer_load_dwordx4 v164, s[16:19], s24 offen offset:1024 lds\n"
    "buffer_load_dwordx4 v165, s[16:19], s24 offen offset:2048 lds\n"
    "s_add_u32 m0, s25, 9216\n"
    "s_add_u32 s24, s24, 55296\n"
    "buffer_load_dwordx4 v163, s[16:19], s24 offen offset:0 lds\n"
    "buffer_load_dwordx4 v164, s[16:19], s24 offen offset:1024 lds\n"
    "buffer_load_dwordx4 v165, s[16:19], s24 offen offset:2048 lds\n"
    "v_mov_b32 v154, 163328\n"
    "v_mov_b32 v155, 1\n"
    "v_bfe_u32 v6, v1, 4, 1\n"
    "v_lshlrev_b32 v6, 14, v6\n"
    "v_and_b32 v7, 15, v1\n"
    "v_lshl_add_u32 v6, v7, 4, v6\n"
    "v_lshrrev_b32 v7, 5, v1\n"
    "v_lshl_add_u32 v6, v7, 8, v6\n"
    "s_waitcnt vmcnt(0)\n"
    "s_mov_b64 exec, 1\n"
    "ds_add_u32 v154, v155\n"
    "s_mov_b64 exec, -1\n"
    "s_mov_b32 s46, 0x100000\n"
    "L_cnt:\n"
    "ds_read_b32 v155, v154\n"
    "s_sub_u32 s46, s46, 1\n"
    "s_waitcnt lgkmcnt(0)\n"
    "v_cmp_gt_u32 vcc, 4, v155\n"
    "s_cbranch_vccz L_cntok\n"
    "s_sleep 1\n"
    "s_cbranch_scc0 L_cnt\n"
    "L_cntok:\n"
    "ds_read_b64_tr_b16 v[8:9], v5 offset:0\n"
    "ds_read_b64_tr_b16 v[10:11], v5 offset:512\n"
    "ds_read_b64_tr_b16 v[12:13], v5 offset:2048\n"
    "ds_read_b64_tr_b16 v[14:15], v5 offset:2560\n"
    "ds_read_b64_tr_b16 v[16:17], v5 offset:4096\n"
    "ds_read_b64_tr_b16 v[18:19], v5 offset:4608\n"
    "ds_read_b64_tr_b16 v[20:21], v5 offset:6144\n"
    "ds_read_b64_tr_b16 v[22:23], v5 offset:6656\n"
    "ds_read_b64_tr_b16 v[24:25], v5 offset:8192\n"
    "ds_read_b64_tr_b16 v[26:27], v5 offset:8704\n"
    "ds_read_b64_tr_b16 v[28:29], v5 offset:10240\n"
    "ds_read_b64_tr_b16 v[30:31], v5 offset:10752\n"
    "ds_read_b64_tr_b16 v[32:33], v5 offset:12288\n"
    "ds_read_b64_tr_b16 v[34:35], v5 offset:12800\n"
    "ds_read_b64_tr_b16 v[36:37], v5 offset:14336\n"
    "ds_read_b64_tr_b16 v[38:39], v5 offset:14848\n"
    "ds_read_b64_tr_b16 v[40:41], v5 offset:16384\n"
    "ds_read_b64_tr_b16 v[42:43], v5 offset:16896\n"
    "ds_read_b64_tr_b16 v[44:45], v5 offset:18432\n"
    "ds_read_b64_tr_b16 v[46:47], v5 offset:18944\n"
    "ds_read_b64_tr_b16 v[48:49], v5 offset:20480\n"
    "ds_read_b64_tr_b16 v[50:51], v5 offset:20992\n"
    "ds_read_b64_tr_b16 v[52:53], v5 offset:22528\n"
    "ds_read_b64_tr_b16 v[54:55], v5 offset:23040\n"
    "ds_read_b64_tr_b16 v[56:57], v5 offset:24576\n"
    "ds_read_b64_tr_b16 v[58:59], v5 offset:25088\n"
    "ds_read_b64_tr_b16 v[60:61], v5 offset:26624\n"
    "ds_read_b64_tr_b16 v[62:63], v5 offset:27136\n"
    "ds_read_b64_tr_b16 v[64:65], v5 offset:28672\n"
    "ds_read_b64_tr_b16 v[66:67], v5 offset:29184\n"
    "ds_read_b64_tr_b16 v[68:69], v5 offset:30720\n"
    "ds_read_b64_tr_b16 v[70:71], v5 offset:31232\n"
    "ds_read_b64_tr_b16 v[72:73], v5 offset:32768\n"
    "ds_read_b64_tr_b16 v[74:75], v5 offset:33280\n"
    "ds_read_b64_tr_b16 v[76:77], v5 offset:34816\n"
    "ds_read_b64_tr_b16 v[78:79], v5 offset:35328\n"
    "ds_read_b64_tr_b16 v[80:81], v5 offset:36864\n"
    "ds_read_b64_tr_b16 v[82:83], v5 offset:37376\n"
    "ds_read_b64_tr_b16 v[84:85], v5 offset:38912\n"
    "ds_read_b64_tr_b16 v[86:87], v5 offset:39424\n"
    "ds_read_b64_tr_b16 v[88:89], v5 offset:40960\n"
    "ds_read_b64_tr_b16 v[90:91], v5 offset:41472\n"
    "ds_read_b64_tr_b16 v[92:93], v5 offset:43008\n"
    "ds_read_b64_tr_b16 v[94:95], v5 offset:43520\n"
    "ds_read_b64_tr_b16 v[96:97], v5 offset:45056\n"
    "ds_read_b64_tr_b16 v[98:99], v5 offset:45568\n"
    "ds_read_b64_tr_b16 v[100:101], v5 offset:47104\n"
    "ds_read_b64_tr_b16 v[102:103], v5 offset:47616\n"
    "ds_read_b128 v[120:123], v150 offset:0\n"
    "ds_read_b128 v[124:127], v151 offset:1024\n"
    "ds_read_b128 v[128:131], v152 offset:2048\n"
    "ds_read_b128 v[132:135], v153 offset:3072\n"
    "ds_read_b128 v[136:139], v150 offset:4096\n"
    "ds_read_b128 v[140:143], v151 offset:5120\n"
    "s_waitcnt lgkmcnt(5)\n"
    "v_mfma_f32_32x32x16_bf16 v[104:119], v[120:123], v[8:11], 0\n"
    "ds_read_b128 v[120:123], v152 offset:6144\n"
    "s_waitcnt lgkmcnt(5)\n"
    "v_mfma_f32_32x32x16_bf16 v[104:119], v[124:127], v[12:15], v[104:119]\n"
    "ds_read_b128 v[124:127], v153 offset:7168\n"
    "s_waitcnt lgkmcnt(5)\n"
    "v_mfma_f32_32x32x16_bf16 v[104:119], v[128:131], v[16:19], v[104:119]\n"
    "ds_read_b128 v[128:131], v150 offset:8192\n"
    "s_waitcnt lgkmcnt(5)\n"
    "v_mfma_f32_32x32x16_bf16 v[104:119], v[132:135], v[20:23], v[104:119]\n"
    "ds_read_b128 v[132:135], v151 offset:9216\n"
    "s_waitcnt lgkmcnt(5)\n"
    "v_mfma_f32_32x32x16_bf16 v[104:119], v[136:139], v[24:27], v[104:119]\n"
    "ds_read_b128 v[136:139], v152 offset:10240\n"
    "s_waitcnt lgkmcnt(5)\n"
    "v_mfma_f32_32x32x16_bf16 v[104:119], v[140:143], v[28:31], v[104:119]\n"
    "ds_read_b128 v[140:143], v153 offset:11264\n"
    "s_waitcnt lgkmcnt(5)\n"
    "v_mfma_f32_32x32x16_bf16 v[104:119], v[120:123], v[32:35], v[104:119]\n"
    "ds_read_b128 v[120:123], v150 offset:12288\n"
    "s_waitcnt lgkmcnt(5)\n"
    "v_mfma_f32_32x32x16_bf16 v[104:119], v[124:127], v[36:39], v[104:119]\n"
    "ds_read_b128 v[124:127], v151 offset:13312\n"
    "s_waitcnt lgkmcnt(5)\n"
    "v_mfma_f32_32x32x16_bf16 v[104:119], v[128:131], v[40:43], v[104:119]\n"
    "ds_read_b128 v[128:131], v152 offset:14336\n"
    "s_waitcnt lgkmcnt(5)\n"
    "v_mfma_f32_32x32x16_bf16 v[104:119], v[132:135], v[44:47], v[104:119]\n"
    "ds_read_b128 v[132:135], v153 offset:15360\n"
    "s_waitcnt lgkmcnt(5)\n"
    "v_mfma_f32_32x32x16_bf16 v[104:119], v[136:139], v[48:51], v[104:119]\n"
    "ds_read_b128 v[136:139], v150 offset:16384\n"
    "s_waitcnt lgkmcnt(5)\n"
    "v_mfma_f32_32x32x16_bf16 v[104:119], v[140:143], v[52:55], v[104:119]\n"
    "ds_read_b128 v[140:143], v151 offset:17408\n"
    "s_waitcnt lgkmcnt(5)\n"
    "v_mfma_f32_32x32x16_bf16 v[104:119], v[120:123], v[56:59], v[104:119]\n"
    "ds_read_b128 v[120:123], v152 offset:18432\n"
    "s_waitcnt lgkmcnt(5)\n"
    "v_mfma_f32_32x32x16_bf16 v[104:119], v[124:127], v[60:63], v[104:119]\n"
    "ds_read_b128 v[124:127], v153 offset:19456\n"
    "s_waitcnt lgkmcnt(5)\n"
    "v_mfma_f32_32x32x16_bf16 v[104:119], v[128:131], v[64:67], v[104:119]\n"
    "ds_read_b128 v[128:131], v150 offset:20480\n"
    "s_waitcnt lgkmcnt(5)\n"
    "v_mfma_f32_32x32x16_bf16 v[104:119], v[132:135], v[68:71], v[104:119]\n"
    "ds_read_b128 v[132:135], v151 offset:21504\n"
    "s_waitcnt lgkmcnt(5)\n"
    "v_mfma_f32_32x32x16_bf16 v[104:119], v[136:139], v[72:75], v[104:119]\n"
    "ds_read_b128 v[136:139], v152 offset:22528\n"
    "s_waitcnt lgkmcnt(5)\n"
    "v_mfma_f32_32x32x16_bf16 v[104:119], v[140:143], v[76:79], v[104:119]\n"
    "ds_read_b128 v[140:143], v153 offset:23552\n"
    "s_waitcnt lgkmcnt(5)\n"
    "v_mfma_f32_32x32x16_bf16 v[104:119], v[120:123], v[80:83], v[104:119]\n"
    "s_waitcnt lgkmcnt(4)\n"
    "v_mfma_f32_32x32x16_bf16 v[104:119], v[124:127], v[84:87], v[104:119]\n"
    "s_waitcnt lgkmcnt(3)\n"
    "v_mfma_f32_32x32x16_bf16 v[104:119], v[128:131], v[88:91], v[104:119]\n"
    "s_waitcnt lgkmcnt(2)\n"
    "v_mfma_f32_32x32x16_bf16 v[104:119], v[132:135], v[92:95], v[104:119]\n"
    "s_waitcnt lgkmcnt(1)\n"
    "v_mfma_f32_32x32x16_bf16 v[104:119], v[136:139], v[96:99], v[104:119]\n"
    "s_waitcnt lgkmcnt(0)\n"
    "v_mfma_f32_32x32x16_bf16 v[104:119], v[140:143], v[100:103], v[104:119]\n"
    "s_nop 11\n"
    "v_cvt_pk_bf16_f32 v8, v104, v105\n"
    "v_cvt_pk_bf16_f32 v9, v106, v107\n"
    "v_cvt_pk_bf16_f32 v10, v108, v109\n"
    "v_cvt_pk_bf16_f32 v11, v110, v111\n"
    "buffer_store_dwordx4 v[8:11], v6, s[40:43], s52 offen sc1\n"
    "v_cvt_pk_bf16_f32 v12, v112, v113\n"
    "v_cvt_pk_bf16_f32 v13, v114, v115\n"
    "v_cvt_pk_bf16_f32 v14, v116, v117\n"
    "v_cvt_pk_bf16_f32 v15, v118, v119\n"
    "buffer_store_dwordx4 v[12:15], v6, s[40:43], s52 offen offset:512 sc1\n"
    "s_barrier\n"
    "s_waitcnt vmcnt(0)\n"
    "s_mov_b64 exec, 1\n"
    "buffer_store_dwordx2 v[2:3], off, s[40:43], s53 sc1\n"
    "s_mov_b64 exec, -1\n"
    "s_branch L_p2b\n"
    "L_crit1:\n"
    "v_mul_u32_u24 v24, 768, v5\n"
    "v_lshl_add_u32 v24, v7, 4, v24\n"
    "v_add_u32 v25, 146432, v24\n"
    "s_and_b32 s56, s59, 1\n"
    "s_mul_i32 s56, s56, 12\n"
    "s_add_u32 s56, s56, s3\n"
    "s_mul_i32 s57, s56, 6144\n"
    "s_lshr_b32 s58, s59, 1\n"
    "s_mul_i32 s33, s58, 48\n"
    "s_add_u32 s33, s33, s56\n"
    "s_lshl_b32 s33, s33, 10\n"
    "s_add_u32 s33, s33, 24117248\n"
    "s_lshl_b32 s58, s58, 7\n"
    "s_mov_b32 m0, s2\n"
    "s_add_u32 s57, s57, s58\n"
    "buffer_load_dwordx4 v24, s[20:23], s57 offen lds\n"
    "buffer_load_dwordx4 v25, s[20:23], s57 offen offset:1024 lds\n"
    "v_lshlrev_b32 v23, 4, v1\n"
    "s_waitcnt vmcnt(0)\n"
    "ds_read_b64_tr_b16 v[88:89], v18 offset:0\n"
    "ds_read_b64_tr_b16 v[90:91], v18 offset:1024\n"
    "ds_read_b64_tr_b16 v[92:93], v19 offset:0\n"
    "ds_read_b64_tr_b16 v[94:95], v19 offset:1024\n"
    "s_add_u32 s56, s33, 0\n"
    "s_add_u32 s57, s33, 24576\n"
    "s_waitcnt lgkmcnt(2)\n"
    "buffer_store_dwordx4 v[88:91], v23, s[40:43], s56 offen sc1\n"
    "s_waitcnt lgkmcnt(0)\n"
    "buffer_store_dwordx4 v[92:95], v23, s[40:43], s57 offen sc1\n"
    "s_branch L_helpend\n"
    "L_help1:\n"
    "s_sub_u32 s56, s59, 36\n"
    "s_mul_i32 s57, s56, 43\n"
    "s_lshr_b32 s57, s57, 8\n"
    "s_mul_i32 s57, s57, 294144\n"
    "s_lshl_b32 s56, s56, 7\n"
    "s_add_u32 s57, s57, s56\n"
    "s_mul_i32 s56, s3, 12288\n"
    "s_add_u32 s57, s57, s56\n"
    "s_sub_u32 s56, s59, 12\n"
    "s_lshl_b32 s56, s56, 7\n"
    "s_mul_i32 s58, s3, 49152\n"
    "s_add_u32 s56, s56, s58\n"
    "s_movk_i32 s58, 768\n"
    "s_cmp_lt_u32 s59, 36\n"
    "s_cselect_b32 s56, s56, s57\n"
    "s_cselect_b32 s58, 3072, s58\n"
    "v_mul_u32_u24 v24, s58, v5\n"
    "v_lshl_add_u32 v24, v7, 4, v24\n"
    "s_lshl_b32 s57, s58, 3\n"
    "s_sub_u32 s57, s57, 1024\n"
    "v_add_u32 v25, s57, v24\n"
    "s_mul_i32 s57, s58, 192\n"
    "s_add_u32 s57, s57, s56\n"
    "s_mov_b32 m0, s2\n"
    "s_waitcnt lgkmcnt(0)\n"
    "buffer_load_dword v166, off, s[40:43], 0 sc1\n"
    "s_cmp_lt_u32 s59, 36\n"
    "s_cselect_b64 s[28:29], s[28:29], s[32:33]\n"
    "s_cmp_ge_u32 s59, 60\n"
    "s_cbranch_scc1 L_helpidle\n"
    "buffer_load_dwordx4 v24, s[28:31], s56 offen lds\n"
    "buffer_load_dwordx4 v25, s[28:31], s56 offen offset:1024 lds\n"
    "s_add_u32 m0, s2, 2048\n"
    "s_sub_u32 s33, s59, 6\n"
    "buffer_load_dwordx4 v24, s[28:31], s57 offen lds\n"
    "buffer_load_dwordx4 v25, s[28:31], s57 offen offset:1024 lds\n"
    "s_mul_i32 s33, s33, 48\n"
    "s_add_u32 s33, s33, s3\n"
    "s_lshl_b32 s33, s33, 10\n"
    "s_add_u32 s33, s33, 24117248\n"
    "v_bfe_u32 v18, v1, 2, 2\n"
    "v_lshrrev_b32 v19, 1, v18\n"
    "v_lshlrev_b32 v18, 7, v18\n"
    "v_lshl_add_u32 v18, v19, 6, v18\n"
    "v_bfe_u32 v19, v1, 4, 1\n"
    "v_lshl_add_u32 v18, v19, 5, v18\n"
    "v_and_b32 v19, 3, v1\n"
    "v_lshl_add_u32 v18, v19, 3, v18\n"
    "v_lshrrev_b32 v19, 5, v1\n"
    "v_lshl_add_u32 v18, v19, 9, v18\n"
    "v_add_u32 v18, s2, v18\n"
    "v_xor_b32 v19, 64, v18\n"
    "v_lshlrev_b32 v23, 4, v1\n"
    "s_waitcnt vmcnt(4)\n"
    "v_readfirstlane_b32 s44, v166\n"
    "s_mul_i32 s44, s44, 1664525\n"
    "s_add_u32 s44, s44, 1013904223\n"
    "s_not_b32 s45, s44\n"
    "v_mov_b32 v2, s44\n"
    "v_mov_b32 v3, s45\n"
    "s_waitcnt vmcnt(0)\n"
    "ds_read_b64_tr_b16 v[88:89], v18 offset:0\n"
    "ds_read_b64_tr_b16 v[90:91], v18 offset:1024\n"
    "ds_read_b64_tr_b16 v[92:93], v19 offset:0\n"
    "ds_read_b64_tr_b16 v[94:95], v19 offset:1024\n"
    "s_add_u32 s56, s33, 0\n"
    "s_add_u32 s57, s33, 24576\n"
    "s_waitcnt lgkmcnt(2)\n"
    "buffer_store_dwordx4 v[88:91], v23, s[40:43], s56 offen sc1\n"
    "s_waitcnt lgkmcnt(0)\n"
    "buffer_store_dwordx4 v[92:95], v23, s[40:43], s57 offen sc1\n"
    "ds_read_b64_tr_b16 v[96:97], v18 offset:2048\n"
    "ds_read_b64_tr_b16 v[98:99], v18 offset:3072\n"
    "ds_read_b64_tr_b16 v[100:101], v19 offset:2048\n"
    "ds_read_b64_tr_b16 v[102:103], v19 offset:3072\n"
    "s_add_u32 s56, s33, 12288\n"
    "s_add_u32 s57, s33, 36864\n"
    "s_waitcnt lgkmcnt(2)\n"
    "buffer_store_dwordx4 v[96:99], v23, s[40:43], s56 offen sc1\n"
    "s_waitcnt lgkmcnt(0)\n"
    "buffer_store_dwordx4 v[100:103], v23, s[40:43], s57 offen sc1\n"
    "L_helpend:\n"
    "s_waitcnt vmcnt(0)\n"
    "s_barrier\n"
    "s_cmp_lg_u32 s3, 0\n"
    "s_cbranch_scc1 L_nofw\n"
    "s_lshl_b32 s56, s59, 7\n"
    "s_add_u32 s56, s56, 262144\n"
    "s_mov_b64 exec, 1\n"
    "buffer_store_dwordx2 v[2:3], off, s[40:43], s56 sc1\n"
    "s_mov_b64 exec, -1\n"
    "L_nofw:\n"
    "s_sub_u32 s56, s59, 12\n"
    "s_cmp_ge_u32 s56, 24\n"
    "s_cbranch_scc1 L_nomodb\n"
    "v_mul_u32_u24 v8, 4608, v5\n"
    "v_lshl_add_u32 v8, v7, 4, v8\n"
    "v_add_u32 v9, 35840, v8\n"
    "v_add_u32 v10, 71680, v8\n"
    "v_add_u32 v11, 107520, v8\n"
    "v_lshrrev_b32 v12, 5, v1\n"
    "v_mul_u32_u24 v12, 704, v12\n"
    "v_lshl_add_u32 v12, v1, 1, v12\n"
    "s_mul_i32 s56, s3, 147456\n"
    "s_lshl_b32 s57, s59, 7\n"
    "s_add_u32 s56, s56, s57\n"
    "s_lshl_b32 s57, s3, 6\n"
    "s_add_u32 m0, s2, 4096\n"
    "s_waitcnt lgkmcnt(0)\n"
    "buffer_load_ushort v13, v12, s[12:15], s57 offen\n"
    "buffer_load_dwordx4 v8, s[24:27], s56 offen offset:0 lds\n"
    "buffer_load_dwordx4 v9, s[24:27], s56 offen offset:1024 lds\n"
    "buffer_load_dwordx4 v10, s[24:27], s56 offen offset:2048 lds\n"
    "buffer_load_dwordx4 v11, s[24:27], s56 offen offset:3072 lds\n"
    "s_lshl_b32 s56, s3, 7\n"
    "s_add_u32 s56, s56, 98304\n"
    "v_lshl_add_u32 v14, v1, 1, s56\n"
    "v_and_b32 v15, 1, v1\n"
    "v_lshrrev_b32 v16, 5, v1\n"
    "v_lshl_add_u32 v15, v15, 3, v16\n"
    "v_lshl_add_u32 v15, v15, 3, s56\n"
    "s_waitcnt vmcnt(4)\n"
    "v_lshlrev_b32 v13, 16, v13\n"
    "v_mul_f32 v17, 0xbfb8aa3b, v13\n"
    "v_exp_f32 v17, v17\n"
    "s_nop 0\n"
    "v_add_f32 v17, 1.0, v17\n"
    "v_rcp_f32 v17, v17\n"
    "s_nop 0\n"
    "v_mul_f32 v13, v13, v17\n"
    "v_cvt_pk_bf16_f32 v13, v13, v13\n"
    "ds_write_b16 v14, v13\n"
    "ds_read_b64 v[24:25], v15 offset:0\n"
    "ds_read_b64 v[26:27], v15 offset:16\n"
    "ds_read_b64 v[28:29], v15 offset:32\n"
    "ds_read_b64 v[30:31], v15 offset:48\n"
    "s_lshl_b32 s56, s3, 9\n"
    "s_add_u32 s56, s56, 100352\n"
    "v_lshl_add_u32 v20, v1, 2, s56\n"
    "v_lshlrev_b32 v21, 3, v1\n"
    "v_mul_u32_u24 v22, 5888, v16\n"
    "v_add_u32 v22, v21, v22\n"
    "v_add_u32 v21, 100352, v21\n"
    "s_lshl_b32 s57, s59, 8\n"
    "s_add_u32 s57, s57, 320512\n"
    "s_sub_u32 s56, s59, 24\n"
    "s_cmp_lt_u32 s56, 6\n"
    "s_cselect_b32 s58, 1.0, 0\n"
    "s_waitcnt vmcnt(0)\n"
    "ds_read_b64_tr_b16 v[32:33], v18 offset:4096\n"
    "ds_read_b64_tr_b16 v[34:35], v18 offset:5120\n"
    "ds_read_b64_tr_b16 v[36:37], v19 offset:4096\n"
    "ds_read_b64_tr_b16 v[38:39], v19 offset:5120\n"
    "ds_read_b64_tr_b16 v[40:41], v18 offset:6144\n"
    "ds_read_b64_tr_b16 v[42:43], v18 offset:7168\n"
    "ds_read_b64_tr_b16 v[44:45], v19 offset:6144\n"
    "ds_read_b64_tr_b16 v[46:47], v19 offset:7168\n"
    "s_waitcnt lgkmcnt(6)\n"
    "v_mfma_f32_32x32x16_bf16 v[56:71], v[24:27], v[32:35], 0\n"
    "s_waitcnt lgkmcnt(4)\n"
    "v_mfma_f32_32x32x16_bf16 v[72:87], v[24:27], v[36:39], 0\n"
    "s_waitcnt lgkmcnt(2)\n"
    "v_mfma_f32_32x32x16_bf16 v[56:71], v[28:31], v[40:43], v[56:71]\n"
    "s_waitcnt lgkmcnt(0)\n"
    "v_mfma_f32_32x32x16_bf16 v[72:87], v[28:31], v[44:47], v[72:87]\n"
    "s_mov_b32 exec_hi, 0\n"
    "s_nop 8\n"
    "ds_write_b32 v20, v56\n"
    "ds_write_b32 v20, v57 offset:256\n"
    "ds_write_b32 v20, v72 offset:128\n"
    "ds_write_b32 v20, v73 offset:384\n"
    "s_mov_b32 exec_hi, -1\n"
    "s_waitcnt lgkmcnt(0)\n"
    "s_barrier\n"
    "s_cmp_ge_u32 s3, 1\n"
    "s_cbranch_scc1 L_modend_8\n"
    "ds_read_b64 v[24:25], v21 offset:0\n"
    "ds_read_b64 v[26:27], v21 offset:512\n"
    "ds_read_b64 v[28:29], v21 offset:1024\n"
    "ds_read_b64 v[30:31], v21 offset:1536\n"
    "ds_read_b64 v[32:33], v21 offset:2048\n"
    "ds_read_b64 v[34:35], v21 offset:2560\n"
    "ds_read_b64 v[36:37], v21 offset:3072\n"
    "ds_read_b64 v[38:39], v21 offset:3584\n"
    "ds_read_b64 v[40:41], v21 offset:4096\n"
    "ds_read_b64 v[42:43], v21 offset:4608\n"
    "ds_read_b64 v[44:45], v21 offset:5120\n"
    "ds_read_b64 v[46:47], v21 offset:5632\n"
    "s_waitcnt lgkmcnt(10)\n"
    "v_pk_add_f32 v[24:25], v[24:25], v[26:27]\n"
    "s_waitcnt lgkmcnt(8)\n"
    "v_pk_add_f32 v[28:29], v[28:29], v[30:31]\n"
    "s_waitcnt lgkmcnt(6)\n"
    "v_pk_add_f32 v[32:33], v[32:33], v[34:35]\n"
    "s_waitcnt lgkmcnt(4)\n"
    "v_pk_add_f32 v[36:37], v[36:37], v[38:39]\n"
    "s_waitcnt lgkmcnt(2)\n"
    "v_pk_add_f32 v[40:41], v[40:41], v[42:43]\n"
    "s_waitcnt lgkmcnt(0)\n"
    "v_pk_add_f32 v[44:45], v[44:45], v[46:47]\n"
    "v_pk_add_f32 v[24:25], v[24:25], v[28:29]\n"
    "v_pk_add_f32 v[32:33], v[32:33], v[36:37]\n"
    "v_pk_add_f32 v[40:41], v[40:41], v[44:45]\n"
    "v_pk_add_f32 v[24:25], v[24:25], v[32:33]\n"
    "v_pk_add_f32 v[24:25], v[24:25], v[40:41]\n"
    "v_cvt_pk_bf16_f32 v28, v24, v25\n"
    "v_lshlrev_b32 v24, 16, v28\n"
    "v_and_b32 v25, 0xffff0000, v28\n"
    "v_add_f32 v24, s58, v24\n"
    "v_add_f32 v25, s58, v25\n"
    "v_cvt_pk_bf16_f32 v28, v24, v25\n"
    "v_mov_b32 v29, s44\n"
    "buffer_store_dwordx2 v[28:29], v22, s[40:43], s57 offen sc1\n"
    "L_modend_8:\n"
    "L_nomodb:\n"
    "s_movk_i32 s47, 4096\n"
    "s_movk_i32 s48, 8192\n"
    "s_movk_i32 s49, 12288\n"
    "s_movk_i32 s50, 16384\n"
    "s_movk_i32 s51, 20480\n"
    "v_lshlrev_b32 v0, 4, v1\n"
    "s_mul_i32 s56, s3, 24576\n"
    "s_add_u32 s56, s56, 24117248\n"
    "v_add_u32 v4, s56, v0\n"
    "s_and_b32 s56, s5, 1\n"
    "s_lshl_b32 s57, s4, 4\n"
    "s_lshl_b32 s58, s56, 3\n"
    "s_or_b32 s57, s57, s58\n"
    "s_lshr_b32 s58, s3, 1\n"
    "s_or_b32 s58, s58, s57\n"
    "s_lshl_b32 s58, s58, 7\n"
    "s_add_u32 s60, s58, 49152\n"
    "s_add_u32 s57, s57, s3\n"
    "s_and_b32 s58, s4, 5\n"
    "s_lshl_b32 s58, s58, 4\n"
    "s_and_b32 s0, s5, 16\n"
    "s_lshl_b32 s0, s0, 1\n"
    "s_or_b32 s58, s58, s0\n"
    "s_and_b32 s0, s5, 14\n"
    "s_or_b32 s58, s58, s0\n"
    "s_bfe_u32 s0, s3, 0x10001\n"
    "s_or_b32 s58, s58, s0\n"
    "s_and_b32 s0, s3, 1\n"
    "s_mul_i32 s1, s0, 6144\n"
    "s_add_u32 s1, s1, 3145728\n"
    "s_mul_i32 s0, s0, 24\n"
    "s_add_u32 s0, s0, 32768\n"
    "s_lshl_b32 s34, s58, 14\n"
    "s_add_u32 s34, s34, s1\n"
    "s_lshl_b32 s35, s58, 7\n"
    "s_add_u32 s35, s35, s0\n"
    "s_lshl_b32 s58, s57, 14\n"
    "s_add_u32 s54, s58, 1048576\n"
    "s_lshl_b32 s58, s57, 7\n"
    "s_add_u32 s55, s58, 16384\n"
    "s_and_b32 s57, s4, 4\n"
    "s_lshl_b32 s57, s57, 2\n"
    "s_lshr_b32 s58, s5, 1\n"
    "s_or_b32 s57, s57, s58\n"
    "s_and_b32 s58, s4, 1\n"
    "s_lshl_b32 s58, s58, 2\n"
    "s_and_b32 s0, s4, 2\n"
    "s_or_b32 s58, s58, s0\n"
    "s_or_b32 s58, s58, s56\n"
    "s_lshl_b32 s0, s57, 4\n"
    "s_or_b32 s1, s0, s58\n"
    "s_lshl_b32 s1, s1, 4\n"
    "s_or_b32 s1, s1, s3\n"
    "s_lshl_b32 s1, s1, 10\n"
    "s_add_u32 s61, s1, 7340032\n"
    "s_or_b32 s0, s0, s3\n"
    "s_lshl_b32 s0, s0, 4\n"
    "s_or_b32 s0, s0, s58\n"
    "s_lshl_b32 s0, s0, 3\n"
    "s_add_u32 s62, s0, 65536\n"
    "s_lshl_b32 s57, s4, 3\n"
    "s_or_b32 s57, s57, s7\n"
    "s_mul_i32 s58, s57, 24576\n"
    "s_lshl_b32 s0, s3, 6\n"
    "s_add_u32 s63, s58, s0\n"
    "s_lshr_b32 s58, s4, 2\n"
    "s_mul_i32 s58, s58, 6144\n"
    "s_lshl_b32 s0, s3, 9\n"
    "s_add_u32 s58, s58, s0\n"
    "s_add_u32 s96, s58, 323584\n"
    "s_lshl_b32 s58, s57, 7\n"
    "s_or_b32 s58, s58, s3\n"
    "s_lshl_b32 s58, s58, 10\n"
    "s_add_u32 s97, s58, 7340032\n"
    "s_lshr_b32 s58, s57, 1\n"
    "s_lshl_b32 s0, s58, 4\n"
    "s_or_b32 s0, s0, s3\n"
    "s_lshl_b32 s0, s0, 7\n"
    "s_add_u32 s98, s0, 65536\n"
    "s_lshl_b32 s58, s58, 4\n"
    "s_and_b32 s0, s57, 1\n"
    "s_or_b32 s58, s58, s0\n"
    "s_lshl_b32 s58, s58, 8\n"
    "s_add_u32 s99, s58, 393216\n"
    "s_branch L_helpdone\n"
    "L_helpidle:\n"
    "s_waitcnt vmcnt(0)\n"
    "v_readfirstlane_b32 s44, v166\n"
    "s_mul_i32 s44, s44, 1664525\n"
    "s_add_u32 s44, s44, 1013904223\n"
    "s_not_b32 s45, s44\n"
    "v_mov_b32 v2, s44\n"
    "v_mov_b32 v3, s45\n"
    "s_movk_i32 s47, 4096\n"
    "s_movk_i32 s48, 8192\n"
    "s_movk_i32 s49, 12288\n"
    "s_movk_i32 s50, 16384\n"
    "s_movk_i32 s51, 20480\n"
    "v_lshlrev_b32 v0, 4, v1\n"
    "s_mul_i32 s56, s3, 24576\n"
    "s_add_u32 s56, s56, 24117248\n"
    "v_add_u32 v4, s56, v0\n"
    "s_and_b32 s56, s5, 1\n"
    "s_lshl_b32 s57, s4, 4\n"
    "s_lshl_b32 s58, s56, 3\n"
    "s_or_b32 s57, s57, s58\n"
    "s_lshr_b32 s58, s3, 1\n"
    "s_or_b32 s58, s58, s57\n"
    "s_lshl_b32 s58, s58, 7\n"
    "s_add_u32 s60, s58, 49152\n"
    "s_add_u32 s57, s57, s3\n"
    "s_and_b32 s58, s4, 5\n"
    "s_lshl_b32 s58, s58, 4\n"
    "s_and_b32 s0, s5, 16\n"
    "s_lshl_b32 s0, s0, 1\n"
    "s_or_b32 s58, s58, s0\n"
    "s_and_b32 s0, s5, 14\n"
    "s_or_b32 s58, s58, s0\n"
    "s_bfe_u32 s0, s3, 0x10001\n"
    "s_or_b32 s58, s58, s0\n"
    "s_and_b32 s0, s3, 1\n"
    "s_mul_i32 s1, s0, 6144\n"
    "s_add_u32 s1, s1, 3145728\n"
    "s_mul_i32 s0, s0, 24\n"
    "s_add_u32 s0, s0, 32768\n"
    "s_lshl_b32 s34, s58, 14\n"
    "s_add_u32 s34, s34, s1\n"
    "s_lshl_b32 s35, s58, 7\n"
    "s_add_u32 s35, s35, s0\n"
    "s_lshl_b32 s58, s57, 14\n"
    "s_add_u32 s54, s58, 1048576\n"
    "s_lshl_b32 s58, s57, 7\n"
    "s_add_u32 s55, s58, 16384\n"
    "s_and_b32 s57, s4, 4\n"
    "s_lshl_b32 s57, s57, 2\n"
    "s_lshr_b32 s58, s5, 1\n"
    "s_or_b32 s57, s57, s58\n"
    "s_and_b32 s58, s4, 1\n"
    "s_lshl_b32 s58, s58, 2\n"
    "s_and_b32 s0, s4, 2\n"
    "s_or_b32 s58, s58, s0\n"
    "s_or_b32 s58, s58, s56\n"
    "s_lshl_b32 s0, s57, 4\n"
    "s_or_b32 s1, s0, s58\n"
    "s_lshl_b32 s1, s1, 4\n"
    "s_or_b32 s1, s1, s3\n"
    "s_lshl_b32 s1, s1, 10\n"
    "s_add_u32 s61, s1, 7340032\n"
    "s_or_b32 s0, s0, s3\n"
    "s_lshl_b32 s0, s0, 4\n"
    "s_or_b32 s0, s0, s58\n"
    "s_lshl_b32 s0, s0, 3\n"
    "s_add_u32 s62, s0, 65536\n"
    "s_lshl_b32 s57, s4, 3\n"
    "s_or_b32 s57, s57, s7\n"
    "s_mul_i32 s58, s57, 24576\n"
    "s_lshl_b32 s0, s3, 6\n"
    "s_add_u32 s63, s58, s0\n"
    "s_lshr_b32 s58, s4, 2\n"
    "s_mul_i32 s58, s58, 6144\n"
    "s_lshl_b32 s0, s3, 9\n"
    "s_add_u32 s58, s58, s0\n"
    "s_add_u32 s96, s58, 323584\n"
    "s_lshl_b32 s58, s57, 7\n"
    "s_or_b32 s58, s58, s3\n"
    "s_lshl_b32 s58, s58, 10\n"
    "s_add_u32 s97, s58, 7340032\n"
    "s_lshr_b32 s58, s57, 1\n"
    "s_lshl_b32 s0, s58, 4\n"
    "s_or_b32 s0, s0, s3\n"
    "s_lshl_b32 s0, s0, 7\n"
    "s_add_u32 s98, s0, 65536\n"
    "s_lshl_b32 s58, s58, 4\n"
    "s_and_b32 s0, s57, 1\n"
    "s_or_b32 s58, s58, s0\n"
    "s_lshl_b32 s58, s58, 8\n"
    "s_add_u32 s99, s58, 393216\n"
    "s_cmp_lg_u32 s59, 60\n"
    "s_cbranch_scc1 L_helpdone\n"
    "s_cmp_lg_u32 s3, 0\n"
    "s_cbranch_scc1 L_helpdone\n"
    "v_min_u32 v5, 59, v1\n"
    "v_lshlrev_b32 v5, 7, v5\n"
    "s_mov_b32 s56, 262144\n"
    "buffer_load_dwordx2 v[156:157], v5, s[40:43], s56 offen sc1\n"
    "s_movk_i32 s46, 32767\n"
    "L_fwh_9:\n"
    "s_sub_u32 s46, s46, 1\n"
    "s_waitcnt vmcnt(0)\n"
    "v_cmp_ne_u64 vcc, s[44:45], v[156:157]\n"
    "s_cbranch_vccz L_fwh_ok_10\n"
    "s_sleep 4\n"
    "buffer_load_dwordx2 v[156:157], v5, s[40:43], s56 offen sc1\n"
    "s_cbranch_scc0 L_fwh_9\n"
    "L_fwh_ok_10:\n"
    "v_lshlrev_b32 v5, 7, v1\n"
    "s_mov_b32 s56, 270336\n"
    "s_mov_b64 exec, 0xff\n"
    "buffer_store_dwordx2 v[2:3], v5, s[40:43], s56 offen sc1\n"
    "s_mov_b64 exec, -1\n"
    "L_helpdone:\n"
    "s_cmp_lt_u32 s3, 8\n"
    "s_cbranch_scc1 L_p2a\n"
    "s_cmp_lg_u32 s3, 8\n"
    "s_cbranch_scc1 L_gate\n"
    "v_min_u32 v5, 5, v1\n"
    "v_lshlrev_b32 v5, 3, v5\n"
    "buffer_load_dwordx2 v[40:41], v5, s[40:43], s35 offen sc1\n"
    "s_movk_i32 s46, 32767\n"
    "L_gate_11:\n"
    "s_sub_u32 s46, s46, 1\n"
    "s_waitcnt vmcnt(0)\n"
    "v_cmp_ne_u64 vcc, s[44:45], v[40:41]\n"
    "s_cbranch_vccz L_gate_ok_12\n"
    "s_sleep 6\n"
    "buffer_load_dwordx2 v[40:41], v5, s[40:43], s35 offen sc1\n"
    "s_cbranch_scc0 L_gate_11\n"
    "L_gate_ok_12:\n"
    "L_gate:\n"
    "s_barrier\n"
    "s_branch L_p2b\n"
    "L_p2a:\n"
    "s_lshl_b32 s56, s4, 4\n"
    "s_or_b32 s56, s56, s3\n"
    "s_lshl_b32 s56, s56, 1\n"
    "s_and_b32 s57, s5, 1\n"
    "s_or_b32 s56, s56, s57\n"
    "s_lshl_b32 s56, s56, 13\n"
    "s_add_u32 s56, s56, 5242880\n"
    "v_add_u32 v6, s56, v0\n"
    "v_and_b32 v5, 7, v1\n"
    "v_lshlrev_b32 v5, 3, v5\n"
    "v_add_u32 v124, s54, v0\n"
    "v_min_u32 v125, 5, v1\n"
    "v_lshlrev_b32 v125, 3, v125\n"
    "s_lshl_b32 s56, s3, 2\n"
    "s_add_u32 s56, s56, 106496\n"
    "v_lshl_add_u32 v128, v1, 5, s56\n"
    "v_and_b32 v129, 31, v1\n"
    "v_lshlrev_b32 v129, 5, v129\n"
    "v_add_u32 v129, 106496, v129\n"
    "v_bfe_u32 v130, v1, 4, 1\n"
    "v_lshlrev_b32 v130, 13, v130\n"
    "v_and_b32 v7, 15, v1\n"
    "v_lshl_add_u32 v130, v7, 4, v130\n"
    "v_lshrrev_b32 v7, 5, v1\n"
    "v_lshl_add_u32 v130, v7, 8, v130\n"
    "s_lshl_b32 s56, s3, 10\n"
    "s_add_u32 s56, s56, 73728\n"
    "v_add_u32 v130, s56, v130\n"
    "v_add_u32 v7, 49152, v0\n"
    "s_barrier\n"
    "s_cmp_ge_u32 s3, 4\n"
    "s_cbranch_scc1 L_noq\n"
    "v_add_u32 v126, s34, v0\n"
    "s_mul_i32 s56, s3, 6144\n"
    "v_add_u32 v127, s56, v7\n"
    "v_min_u32 v134, 2, v1\n"
    "v_lshlrev_b32 v134, 3, v134\n"
    "buffer_load_dwordx2 v[56:57], v134, s[40:43], s35 offen sc1\n"
    "s_movk_i32 s46, 32767\n"
    "L_fq_13:\n"
    "s_sub_u32 s46, s46, 1\n"
    "s_waitcnt vmcnt(0)\n"
    "v_cmp_ne_u64 vcc, s[44:45], v[56:57]\n"
    "s_cbranch_vccz L_fq_ok_14\n"
    "buffer_load_dwordx2 v[56:57], v134, s[40:43], s35 offen sc1\n"
    "s_cbranch_scc0 L_fq_13\n"
    "L_fq_ok_14:\n"
    "s_waitcnt vmcnt(0)\n"
    "buffer_load_dwordx4 v[56:59], v126, s[40:43], 0 offen offset:0 sc1\n"
    "buffer_load_dwordx4 v[60:63], v126, s[40:43], 0 offen offset:1024 sc1\n"
    "buffer_load_dwordx4 v[64:67], v126, s[40:43], 0 offen offset:2048 sc1\n"
    "buffer_load_dwordx4 v[68:71], v126, s[40:43], 0 offen offset:3072 sc1\n"
    "buffer_load_dwordx4 v[120:123], v126, s[40:43], s47 offen offset:0 sc1\n"
    "buffer_load_dwordx4 v[116:119], v126, s[40:43], s47 offen offset:1024 sc1\n"
    "buffer_load_dwordx2 v[8:9], v125, s[40:43], s55 offen sc1\n"
    "s_waitcnt vmcnt(6)\n"
    "ds_write_b128 v127, v[56:59] offset:0\n"
    "s_waitcnt vmcnt(5)\n"
    "ds_write_b128 v127, v[60:63] offset:1024\n"
    "s_waitcnt vmcnt(4)\n"
    "ds_write_b128 v127, v[64:67] offset:2048\n"
    "s_waitcnt vmcnt(3)\n"
    "ds_write_b128 v127, v[68:71] offset:3072\n"
    "s_waitcnt vmcnt(2)\n"
    "ds_write_b128 v127, v[120:123] offset:4096\n"
    "s_waitcnt vmcnt(1)\n"
    "ds_write_b128 v127, v[116:119] offset:5120\n"
    "s_waitcnt lgkmcnt(0)\n"
    "s_barrier\n"
    "s_waitcnt vmcnt(0)\n"
    "v_cmp_ne_u64 vcc, s[44:45], v[8:9]\n"
    "s_cbranch_vccz L_fkok\n"
    "buffer_load_dwordx2 v[8:9], v125, s[40:43], s55 offen sc1\n"
    "s_movk_i32 s46, 32767\n"
    "L_fk0_15:\n"
    "s_sub_u32 s46, s46, 1\n"
    "s_waitcnt vmcnt(0)\n"
    "v_cmp_ne_u64 vcc, s[44:45], v[8:9]\n"
    "s_cbranch_vccz L_fk0_ok_16\n"
    "buffer_load_dwordx2 v[8:9], v125, s[40:43], s55 offen sc1\n"
    "s_cbranch_scc0 L_fk0_15\n"
    "L_fk0_ok_16:\n"
    "L_fkok:\n"
    "s_waitcnt vmcnt(0)\n"
    "buffer_load_dwordx4 v[8:11], v124, s[40:43], 0 offen offset:0 sc1\n"
    "buffer_load_dwordx4 v[12:15], v124, s[40:43], 0 offen offset:1024 sc1\n"
    "buffer_load_dwordx4 v[16:19], v124, s[40:43], 0 offen offset:2048 sc1\n"
    "buffer_load_dwordx4 v[20:23], v124, s[40:43], 0 offen offset:3072 sc1\n"
    "buffer_load_dwordx4 v[24:27], v124, s[40:43], s47 offen offset:0 sc1\n"
    "buffer_load_dwordx4 v[28:31], v124, s[40:43], s47 offen offset:1024 sc1\n"
    "buffer_load_dwordx2 v[132:133], v5, s[40:43], s60 offen sc1\n"
    "buffer_load_dwordx4 v[32:35], v124, s[40:43], s47 offen offset:2048 sc1\n"
    "buffer_load_dwordx4 v[36:39], v124, s[40:43], s47 offen offset:3072 sc1\n"
    "buffer_load_dwordx4 v[40:43], v124, s[40:43], s48 offen offset:0 sc1\n"
    "buffer_load_dwordx4 v[44:47], v124, s[40:43], s48 offen offset:1024 sc1\n"
    "buffer_load_dwordx4 v[48:51], v124, s[40:43], s48 offen offset:2048 sc1\n"
    "buffer_load_dwordx4 v[52:55], v124, s[40:43], s48 offen offset:3072 sc1\n"
    "s_lshl_b32 s59, s4, 7\n"
    "s_add_u32 s59, s59, 270336\n"
    "buffer_load_dwordx2 v[134:135], off, s[40:43], s59 sc1\n"
    "s_branch L_kq\n"
    "L_noq:\n"
    "buffer_load_dwordx2 v[56:57], v125, s[40:43], s55 offen sc1\n"
    "s_movk_i32 s46, 32767\n"
    "L_fk_17:\n"
    "s_sub_u32 s46, s46, 1\n"
    "s_waitcnt vmcnt(0)\n"
    "v_cmp_ne_u64 vcc, s[44:45], v[56:57]\n"
    "s_cbranch_vccz L_fk_ok_18\n"
    "buffer_load_dwordx2 v[56:57], v125, s[40:43], s55 offen sc1\n"
    "s_cbranch_scc0 L_fk_17\n"
    "L_fk_ok_18:\n"
    "s_sleep 3\n"
    "buffer_load_dwordx4 v[8:11], v124, s[40:43], 0 offen offset:0 sc1\n"
    "buffer_load_dwordx4 v[12:15], v124, s[40:43], 0 offen offset:1024 sc1\n"
    "buffer_load_dwordx4 v[16:19], v124, s[40:43], 0 offen offset:2048 sc1\n"
    "buffer_load_dwordx4 v[20:23], v124, s[40:43], 0 offen offset:3072 sc1\n"
    "buffer_load_dwordx4 v[24:27], v124, s[40:43], s47 offen offset:0 sc1\n"
    "buffer_load_dwordx4 v[28:31], v124, s[40:43], s47 offen offset:1024 sc1\n"
    "buffer_load_dwordx2 v[132:133], v5, s[40:43], s60 offen sc1\n"
    "buffer_load_dwordx4 v[32:35], v124, s[40:43], s47 offen offset:2048 sc1\n"
    "buffer_load_dwordx4 v[36:39], v124, s[40:43], s47 offen offset:3072 sc1\n"
    "buffer_load_dwordx4 v[40:43], v124, s[40:43], s48 offen offset:0 sc1\n"
    "buffer_load_dwordx4 v[44:47], v124, s[40:43], s48 offen offset:1024 sc1\n"
    "buffer_load_dwordx4 v[48:51], v124, s[40:43], s48 offen offset:2048 sc1\n"
    "buffer_load_dwordx4 v[52:55], v124, s[40:43], s48 offen offset:3072 sc1\n"
    "s_lshl_b32 s59, s4, 7\n"
    "s_add_u32 s59, s59, 270336\n"
    "buffer_load_dwordx2 v[134:135], off, s[40:43], s59 sc1\n"
    "s_barrier\n"
    "L_kq:\n"
    "s_waitcnt vmcnt(14)\n"
    "ds_read_b128 v[56:59], v7 offset:0\n"
    "ds_read_b128 v[60:63], v7 offset:12288\n"
    "ds_read_b128 v[64:67], v7 offset:1024\n"
    "ds_read_b128 v[68:71], v7 offset:13312\n"
    "s_waitcnt vmcnt(13) lgkmcnt(3)\n"
    "v_mfma_f32_32x32x16_bf16 v[136:151], v[8:11], v[56:59], 0\n"
    "s_waitcnt lgkmcnt(2)\n"
    "v_mfma_f32_32x32x16_bf16 v[152:167], v[8:11], v[60:63], 0\n"
    "ds_read_b128 v[56:59], v7 offset:2048\n"
    "ds_read_b128 v[60:63], v7 offset:14336\n"
    "s_waitcnt vmcnt(12) lgkmcnt(3)\n"
    "v_mfma_f32_32x32x16_bf16 v[136:151], v[12:15], v[64:67], v[136:151]\n"
    "s_waitcnt lgkmcnt(2)\n"
    "v_mfma_f32_32x32x16_bf16 v[152:167], v[12:15], v[68:71], v[152:167]\n"
    "ds_read_b128 v[64:67], v7 offset:3072\n"
    "ds_read_b128 v[68:71], v7 offset:15360\n"
    "s_waitcnt vmcnt(11) lgkmcnt(3)\n"
    "v_mfma_f32_32x32x16_bf16 v[136:151], v[16:19], v[56:59], v[136:151]\n"
    "s_waitcnt lgkmcnt(2)\n"
    "v_mfma_f32_32x32x16_bf16 v[152:167], v[16:19], v[60:63], v[152:167]\n"
    "ds_read_b128 v[56:59], v7 offset:4096\n"
    "ds_read_b128 v[60:63], v7 offset:16384\n"
    "s_waitcnt vmcnt(10) lgkmcnt(3)\n"
    "v_mfma_f32_32x32x16_bf16 v[136:151], v[20:23], v[64:67], v[136:151]\n"
    "s_waitcnt lgkmcnt(2)\n"
    "v_mfma_f32_32x32x16_bf16 v[152:167], v[20:23], v[68:71], v[152:167]\n"
    "ds_read_b128 v[64:67], v7 offset:5120\n"
    "ds_read_b128 v[68:71], v7 offset:17408\n"
    "s_waitcnt vmcnt(9) lgkmcnt(3)\n"
    "v_mfma_f32_32x32x16_bf16 v[136:151], v[24:27], v[56:59], v[136:151]\n"
    "s_waitcnt lgkmcnt(2)\n"
    "v_mfma_f32_32x32x16_bf16 v[152:167], v[24:27], v[60:63], v[152:167]\n"
    "ds_read_b128 v[56:59], v7 offset:6144\n"
    "ds_read_b128 v[60:63], v7 offset:18432\n"
    "s_waitcnt vmcnt(8) lgkmcnt(3)\n"
    "v_mfma_f32_32x32x16_bf16 v[136:151], v[28:31], v[64:67], v[136:151]\n"
    "s_waitcnt lgkmcnt(2)\n"
    "v_mfma_f32_32x32x16_bf16 v[152:167], v[28:31], v[68:71], v[152:167]\n"
    "ds_read_b128 v[64:67], v7 offset:7168\n"
    "ds_read_b128 v[68:71], v7 offset:19456\n"
    "s_waitcnt vmcnt(6) lgkmcnt(3)\n"
    "v_mfma_f32_32x32x16_bf16 v[136:151], v[32:35], v[56:59], v[136:151]\n"
    "s_waitcnt lgkmcnt(2)\n"
    "v_mfma_f32_32x32x16_bf16 v[152:167], v[32:35], v[60:63], v[152:167]\n"
    "ds_read_b128 v[56:59], v7 offset:8192\n"
    "ds_read_b128 v[60:63], v7 offset:20480\n"
    "s_waitcnt vmcnt(5) lgkmcnt(3)\n"
    "v_mfma_f32_32x32x16_bf16 v[136:151], v[36:39], v[64:67], v[136:151]\n"
    "s_waitcnt lgkmcnt(2)\n"
    "v_mfma_f32_32x32x16_bf16 v[152:167], v[36:39], v[68:71], v[152:167]\n"
    "ds_read_b128 v[64:67], v7 offset:9216\n"
    "ds_read_b128 v[68:71], v7 offset:21504\n"
    "s_waitcnt vmcnt(4) lgkmcnt(3)\n"
    "v_mfma_f32_32x32x16_bf16 v[136:151], v[40:43], v[56:59], v[136:151]\n"
    "s_waitcnt lgkmcnt(2)\n"
    "v_mfma_f32_32x32x16_bf16 v[152:167], v[40:43], v[60:63], v[152:167]\n"
    "ds_read_b128 v[56:59], v7 offset:10240\n"
    "ds_read_b128 v[60:63], v7 offset:22528\n"
    "s_waitcnt vmcnt(3) lgkmcnt(3)\n"
    "v_mfma_f32_32x32x16_bf16 v[136:151], v[44:47], v[64:67], v[136:151]\n"
    "s_waitcnt lgkmcnt(2)\n"
    "v_mfma_f32_32x32x16_bf16 v[152:167], v[44:47], v[68:71], v[152:167]\n"
    "ds_read_b128 v[64:67], v7 offset:11264\n"
    "ds_read_b128 v[68:71], v7 offset:23552\n"
    "s_waitcnt vmcnt(2) lgkmcnt(3)\n"
    "v_mfma_f32_32x32x16_bf16 v[136:151], v[48:51], v[56:59], v[136:151]\n"
    "s_waitcnt lgkmcnt(2)\n"
    "v_mfma_f32_32x32x16_bf16 v[152:167], v[48:51], v[60:63], v[152:167]\n"
    "s_waitcnt vmcnt(1) lgkmcnt(1)\n"
    "v_mfma_f32_32x32x16_bf16 v[136:151], v[52:55], v[64:67], v[136:151]\n"
    "s_waitcnt lgkmcnt(0)\n"
    "v_mfma_f32_32x32x16_bf16 v[152:167], v[52:55], v[68:71], v[152:167]\n"
    "v_cmp_ne_u64 vcc, s[44:45], v[132:133]\n"
    "s_cbranch_vccz L_fvok\n"
    "buffer_load_dwordx2 v[132:133], v5, s[40:43], s60 offen sc1\n"
    "s_movk_i32 s46, 32767\n"
    "L_fv_19:\n"
    "s_sub_u32 s46, s46, 1\n"
    "s_waitcnt vmcnt(0)\n"
    "v_cmp_ne_u64 vcc, s[44:45], v[132:133]\n"
    "s_cbranch_vccz L_fv_ok_20\n"
    "buffer_load_dwordx2 v[132:133], v5, s[40:43], s60 offen sc1\n"
    "s_cbranch_scc0 L_fv_19\n"
    "L_fv_ok_20:\n"
    "L_fvok:\n"
    "buffer_load_dwordx4 v[8:11], v6, s[40:43], 0 offen offset:0 sc1\n"
    "buffer_load_dwordx4 v[12:15], v6, s[40:43], 0 offen offset:1024 sc1\n"
    "buffer_load_dwordx4 v[16:19], v6, s[40:43], 0 offen offset:2048 sc1\n"
    "buffer_load_dwordx4 v[20:23], v6, s[40:43], 0 offen offset:3072 sc1\n"
    "buffer_load_dwordx4 v[24:27], v6, s[40:43], s47 offen offset:0 sc1\n"
    "buffer_load_dwordx4 v[28:31], v6, s[40:43], s47 offen offset:1024 sc1\n"
    "buffer_load_dwordx4 v[32:35], v6, s[40:43], s47 offen offset:2048 sc1\n"
    "buffer_load_dwordx4 v[36:39], v6, s[40:43], s47 offen offset:3072 sc1\n"
    "s_mov_b32 s56, 0x3dd53b95\n"
    "s_mov_b32 s58, 0x3f803f80\n"
    "s_waitcnt vmcnt(8)\n"
    "v_mov_b32 v132, 0\n"
    "v_mov_b32 v133, 0\n"
    "v_max3_f32 v56, v136, v137, v138\n"
    "v_max3_f32 v58, v139, v140, v141\n"
    "v_max3_f32 v56, v56, v142, v143\n"
    "v_max3_f32 v58, v58, v144, v145\n"
    "v_max3_f32 v56, v56, v146, v147\n"
    "v_max3_f32 v58, v58, v148, v149\n"
    "v_max3_f32 v56, v56, v150, v151\n"
    "v_max_f32 v56, v56, v58\n"
    "v_max3_f32 v57, v152, v153, v154\n"
    "v_max3_f32 v59, v155, v156, v157\n"
    "v_max3_f32 v57, v57, v158, v159\n"
    "v_max3_f32 v59, v59, v160, v161\n"
    "v_max3_f32 v57, v57, v162, v163\n"
    "v_max3_f32 v59, v59, v164, v165\n"
    "v_max3_f32 v57, v57, v166, v167\n"
    "v_max_f32 v57, v57, v59\n"
    "s_nop 1\n"
    "v_permlane32_swap_b32 v56, v57\n"
    "v_max_f32 v56, v56, v57\n"
    "ds_write_b32 v128, v56\n"
    "s_waitcnt lgkmcnt(0)\n"
    "s_barrier\n"
    "ds_read_b128 v[56:59], v129 offset:0\n"
    "ds_read_b128 v[60:63], v129 offset:16\n"
    "ds_read_b128 v[64:67], v129 offset:1024\n"
    "ds_read_b128 v[68:71], v129 offset:1040\n"
    "s_waitcnt lgkmcnt(3)\n"
    "v_max3_f32 v56, v56, v57, v58\n"
    "s_waitcnt lgkmcnt(2)\n"
    "v_max3_f32 v59, v59, v60, v61\n"
    "v_max3_f32 v56, v56, v62, v63\n"
    "v_max_f32 v56, v56, v59\n"
    "v_mul_f32 v56, s56, v56\n"
    "v_pk_fma_f32 v[136:137], v[136:137], s[56:57], v[56:57] op_sel_hi:[1,0,0] neg_lo:[0,0,1] neg_hi:[0,0,1]\n"
    "v_pk_fma_f32 v[138:139], v[138:139], s[56:57], v[56:57] op_sel_hi:[1,0,0] neg_lo:[0,0,1] neg_hi:[0,0,1]\n"
    "v_pk_fma_f32 v[140:141], v[140:141], s[56:57], v[56:57] op_sel_hi:[1,0,0] neg_lo:[0,0,1] neg_hi:[0,0,1]\n"
    "v_pk_fma_f32 v[142:143], v[142:143], s[56:57], v[56:57] op_sel_hi:[1,0,0] neg_lo:[0,0,1] neg_hi:[0,0,1]\n"
    "v_pk_fma_f32 v[144:145], v[144:145], s[56:57], v[56:57] op_sel_hi:[1,0,0] neg_lo:[0,0,1] neg_hi:[0,0,1]\n"
    "v_pk_fma_f32 v[146:147], v[146:147], s[56:57], v[56:57] op_sel_hi:[1,0,0] neg_lo:[0,0,1] neg_hi:[0,0,1]\n"
    "v_pk_fma_f32 v[148:149], v[148:149], s[56:57], v[56:57] op_sel_hi:[1,0,0] neg_lo:[0,0,1] neg_hi:[0,0,1]\n"
    "v_pk_fma_f32 v[150:151], v[150:151], s[56:57], v[56:57] op_sel_hi:[1,0,0] neg_lo:[0,0,1] neg_hi:[0,0,1]\n"
    "v_exp_f32 v136, v136\n"
    "v_exp_f32 v137, v137\n"
    "v_exp_f32 v138, v138\n"
    "v_exp_f32 v139, v139\n"
    "v_exp_f32 v140, v140\n"
    "v_exp_f32 v141, v141\n"
    "v_exp_f32 v142, v142\n"
    "v_exp_f32 v143, v143\n"
    "v_cvt_pk_bf16_f32 v40, v136, v137\n"
    "v_cvt_pk_bf16_f32 v41, v138, v139\n"
    "v_cvt_pk_bf16_f32 v42, v140, v141\n"
    "v_cvt_pk_bf16_f32 v43, v142, v143\n"
    "ds_write_b128 v130, v[40:43] offset:0\n"
    "v_exp_f32 v144, v144\n"
    "v_exp_f32 v145, v145\n"
    "v_exp_f32 v146, v146\n"
    "v_exp_f32 v147, v147\n"
    "v_exp_f32 v148, v148\n"
    "v_exp_f32 v149, v149\n"
    "v_exp_f32 v150, v150\n"
    "v_exp_f32 v151, v151\n"
    "v_dot2c_f32_bf16 v132, s58, v40\n"
    "v_dot2c_f32_bf16 v132, s58, v41\n"
    "v_dot2c_f32_bf16 v132, s58, v42\n"
    "v_dot2c_f32_bf16 v132, s58, v43\n"
    "v_cvt_pk_bf16_f32 v44, v144, v145\n"
    "v_cvt_pk_bf16_f32 v45, v146, v147\n"
    "v_cvt_pk_bf16_f32 v46, v148, v149\n"
    "v_cvt_pk_bf16_f32 v47, v150, v151\n"
    "ds_write_b128 v130, v[44:47] offset:512\n"
    "v_dot2c_f32_bf16 v132, s58, v44\n"
    "v_dot2c_f32_bf16 v132, s58, v45\n"
    "v_dot2c_f32_bf16 v132, s58, v46\n"
    "v_dot2c_f32_bf16 v132, s58, v47\n"
    "s_waitcnt lgkmcnt(3)\n"
    "v_max3_f32 v64, v64, v65, v66\n"
    "s_waitcnt lgkmcnt(2)\n"
    "v_max3_f32 v67, v67, v68, v69\n"
    "v_max3_f32 v64, v64, v70, v71\n"
    "v_max_f32 v64, v64, v67\n"
    "v_mul_f32 v64, s56, v64\n"
    "v_pk_fma_f32 v[152:153], v[152:153], s[56:57], v[64:65] op_sel_hi:[1,0,0] neg_lo:[0,0,1] neg_hi:[0,0,1]\n"
    "v_pk_fma_f32 v[154:155], v[154:155], s[56:57], v[64:65] op_sel_hi:[1,0,0] neg_lo:[0,0,1] neg_hi:[0,0,1]\n"
    "v_pk_fma_f32 v[156:157], v[156:157], s[56:57], v[64:65] op_sel_hi:[1,0,0] neg_lo:[0,0,1] neg_hi:[0,0,1]\n"
    "v_pk_fma_f32 v[158:159], v[158:159], s[56:57], v[64:65] op_sel_hi:[1,0,0] neg_lo:[0,0,1] neg_hi:[0,0,1]\n"
    "v_pk_fma_f32 v[160:161], v[160:161], s[56:57], v[64:65] op_sel_hi:[1,0,0] neg_lo:[0,0,1] neg_hi:[0,0,1]\n"
    "v_pk_fma_f32 v[162:163], v[162:163], s[56:57], v[64:65] op_sel_hi:[1,0,0] neg_lo:[0,0,1] neg_hi:[0,0,1]\n"
    "v_pk_fma_f32 v[164:165], v[164:165], s[56:57], v[64:65] op_sel_hi:[1,0,0] neg_lo:[0,0,1] neg_hi:[0,0,1]\n"
    "v_pk_fma_f32 v[166:167], v[166:167], s[56:57], v[64:65] op_sel_hi:[1,0,0] neg_lo:[0,0,1] neg_hi:[0,0,1]\n"
    "v_exp_f32 v152, v152\n"
    "v_exp_f32 v153, v153\n"
    "v_exp_f32 v154, v154\n"
    "v_exp_f32 v155, v155\n"
    "v_exp_f32 v156, v156\n"
    "v_exp_f32 v157, v157\n"
    "v_exp_f32 v158, v158\n"
    "v_exp_f32 v159, v159\n"
    "v_cvt_pk_bf16_f32 v48, v152, v153\n"
    "v_cvt_pk_bf16_f32 v49, v154, v155\n"
    "v_cvt_pk_bf16_f32 v50, v156, v157\n"
    "v_cvt_pk_bf16_f32 v51, v158, v159\n"
    "ds_write_b128 v130, v[48:51] offset:16384\n"
    "v_exp_f32 v160, v160\n"
    "v_exp_f32 v161, v161\n"
    "v_exp_f32 v162, v162\n"
    "v_exp_f32 v163, v163\n"
    "v_exp_f32 v164, v164\n"
    "v_exp_f32 v165, v165\n"
    "v_exp_f32 v166, v166\n"
    "v_exp_f32 v167, v167\n"
    "v_dot2c_f32_bf16 v133, s58, v48\n"
    "v_dot2c_f32_bf16 v133, s58, v49\n"
    "v_dot2c_f32_bf16 v133, s58, v50\n"
    "v_dot2c_f32_bf16 v133, s58, v51\n"
    "v_cvt_pk_bf16_f32 v52, v160, v161\n"
    "v_cvt_pk_bf16_f32 v53, v162, v163\n"
    "v_cvt_pk_bf16_f32 v54, v164, v165\n"
    "v_cvt_pk_bf16_f32 v55, v166, v167\n"
    "ds_write_b128 v130, v[52:55] offset:16896\n"
    "v_dot2c_f32_bf16 v133, s58, v52\n"
    "v_dot2c_f32_bf16 v133, s58, v53\n"
    "v_dot2c_f32_bf16 v133, s58, v54\n"
    "v_dot2c_f32_bf16 v133, s58, v55\n"
    "s_nop 2\n"
    "v_permlane32_swap_b32 v132, v133\n"
    "v_add_f32 v132, v132, v133\n"
    "ds_write_b32 v128, v132 offset:2048\n"
    "v_cmp_ne_u64 vcc, s[44:45], v[134:135]\n"
    "s_cbranch_vccz L_awa_ok_22\n"
    "buffer_load_dwordx2 v[134:135], off, s[40:43], s59 sc1\n"
    "s_movk_i32 s46, 32767\n"
    "L_awa_21:\n"
    "s_sub_u32 s46, s46, 1\n"
    "s_waitcnt vmcnt(0)\n"
    "v_cmp_ne_u64 vcc, s[44:45], v[134:135]\n"
    "s_cbranch_vccz L_awa_ok_22\n"
    "s_sleep 2\n"
    "buffer_load_dwordx2 v[134:135], off, s[40:43], s59 sc1\n"
    "s_cbranch_scc0 L_awa_21\n"
    "L_awa_ok_22:\n"
    "buffer_load_dwordx4 v[72:75], v4, s[40:43], 0 offen offset:0 sc1\n"
    "buffer_load_dwordx4 v[76:79], v4, s[40:43], 0 offen offset:1024 sc1\n"
    "buffer_load_dwordx4 v[80:83], v4, s[40:43], 0 offen offset:2048 sc1\n"
    "buffer_load_dwordx4 v[84:87], v4, s[40:43], 0 offen offset:3072 sc1\n"
    "s_waitcnt lgkmcnt(0)\n"
    "s_barrier\n"
    "s_branch L_pv\n"
    "L_p2b:\n"
    "s_lshl_b32 s56, s4, 4\n"
    "s_or_b32 s56, s56, s3\n"
    "s_lshl_b32 s56, s56, 1\n"
    "s_and_b32 s57, s5, 1\n"
    "s_or_b32 s56, s56, s57\n"
    "s_lshl_b32 s56, s56, 13\n"
    "s_add_u32 s56, s56, 5242880\n"
    "v_add_u32 v6, s56, v0\n"
    "v_and_b32 v5, 7, v1\n"
    "v_lshlrev_b32 v5, 3, v5\n"
    "v_add_u32 v7, 49152, v0\n"
    "s_lshl_b32 s59, s4, 7\n"
    "s_add_u32 s59, s59, 270336\n"
    "buffer_load_dwordx2 v[44:45], off, s[40:43], s59 sc1\n"
    "s_waitcnt vmcnt(0)\n"
    "v_cmp_ne_u64 vcc, s[44:45], v[44:45]\n"
    "s_cbranch_vccz L_awb_ok_24\n"
    "buffer_load_dwordx2 v[44:45], off, s[40:43], s59 sc1\n"
    "s_movk_i32 s46, 32767\n"
    "L_awb_23:\n"
    "s_sub_u32 s46, s46, 1\n"
    "s_waitcnt vmcnt(0)\n"
    "v_cmp_ne_u64 vcc, s[44:45], v[44:45]\n"
    "s_cbranch_vccz L_awb_ok_24\n"
    "s_sleep 2\n"
    "buffer_load_dwordx2 v[44:45], off, s[40:43], s59 sc1\n"
    "s_cbranch_scc0 L_awb_23\n"
    "L_awb_ok_24:\n"
    "s_barrier\n"
    "buffer_load_dwordx2 v[40:41], v5, s[40:43], s60 offen sc1\n"
    "s_movk_i32 s46, 32767\n"
    "L_fvb_25:\n"
    "s_sub_u32 s46, s46, 1\n"
    "s_waitcnt vmcnt(0)\n"
    "v_cmp_ne_u64 vcc, s[44:45], v[40:41]\n"
    "s_cbranch_vccz L_fvb_ok_26\n"
    "s_sleep 1\n"
    "buffer_load_dwordx2 v[40:41], v5, s[40:43], s60 offen sc1\n"
    "s_cbranch_scc0 L_fvb_25\n"
    "L_fvb_ok_26:\n"
    "s_sleep 1\n"
    "buffer_load_dwordx4 v[8:11], v6, s[40:43], 0 offen offset:0 sc1\n"
    "buffer_load_dwordx4 v[12:15], v6, s[40:43], 0 offen offset:1024 sc1\n"
    "buffer_load_dwordx4 v[16:19], v6, s[40:43], 0 offen offset:2048 sc1\n"
    "buffer_load_dwordx4 v[20:23], v6, s[40:43], 0 offen offset:3072 sc1\n"
    "buffer_load_dwordx4 v[24:27], v6, s[40:43], s47 offen offset:0 sc1\n"
    "buffer_load_dwordx4 v[28:31], v6, s[40:43], s47 offen offset:1024 sc1\n"
    "buffer_load_dwordx4 v[32:35], v6, s[40:43], s47 offen offset:2048 sc1\n"
    "buffer_load_dwordx4 v[36:39], v6, s[40:43], s47 offen offset:3072 sc1\n"
    "s_barrier\n"
    "buffer_load_dwordx4 v[72:75], v4, s[40:43], 0 offen offset:0 sc1\n"
    "buffer_load_dwordx4 v[76:79], v4, s[40:43], 0 offen offset:1024 sc1\n"
    "buffer_load_dwordx4 v[80:83], v4, s[40:43], 0 offen offset:2048 sc1\n"
    "buffer_load_dwordx4 v[84:87], v4, s[40:43], 0 offen offset:3072 sc1\n"
    "s_barrier\n"
    "L_pv:\n"
    "s_waitcnt vmcnt(12)\n"
    "ds_read_b128 v[40:43], v7 offset:24576\n"
    "ds_read_b128 v[44:47], v7 offset:32768\n"
    "ds_read_b128 v[48:51], v7 offset:25600\n"
    "s_add_u32 s56, s61, 0x20000\n"
    "ds_read_b128 v[52:55], v7 offset:33792\n"
    "s_waitcnt vmcnt(11) lgkmcnt(3)\n"
    "v_mfma_f32_16x16x32_bf16 v[56:59], v[8:11], v[40:43], 0\n"
    "ds_read_b128 v[40:43], v7 offset:26624\n"
    "s_waitcnt lgkmcnt(3)\n"
    "v_mfma_f32_16x16x32_bf16 v[60:63], v[8:11], v[44:47], 0\n"
    "buffer_load_dwordx4 v[88:91], v4, s[40:43], s47 offen offset:0 sc1\n"
    "ds_read_b128 v[44:47], v7 offset:34816\n"
    "s_waitcnt vmcnt(11) lgkmcnt(3)\n"
    "v_mfma_f32_16x16x32_bf16 v[56:59], v[12:15], v[48:51], v[56:59]\n"
    "ds_read_b128 v[48:51], v7 offset:27648\n"
    "s_waitcnt lgkmcnt(3)\n"
    "v_mfma_f32_16x16x32_bf16 v[60:63], v[12:15], v[52:55], v[60:63]\n"
    "ds_read_b128 v[52:55], v7 offset:35840\n"
    "s_waitcnt vmcnt(10) lgkmcnt(3)\n"
    "v_mfma_f32_16x16x32_bf16 v[56:59], v[16:19], v[40:43], v[56:59]\n"
    "ds_read_b128 v[40:43], v7 offset:28672\n"
    "s_waitcnt lgkmcnt(3)\n"
    "v_mfma_f32_16x16x32_bf16 v[60:63], v[16:19], v[44:47], v[60:63]\n"
    "ds_read_b128 v[44:47], v7 offset:36864\n"
    "s_waitcnt vmcnt(9) lgkmcnt(3)\n"
    "v_mfma_f32_16x16x32_bf16 v[56:59], v[20:23], v[48:51], v[56:59]\n"
    "buffer_load_dwordx4 v[92:95], v4, s[40:43], s47 offen offset:1024 sc1\n"
    "ds_read_b128 v[48:51], v7 offset:29696\n"
    "s_waitcnt lgkmcnt(3)\n"
    "v_mfma_f32_16x16x32_bf16 v[60:63], v[20:23], v[52:55], v[60:63]\n"
    "ds_read_b128 v[52:55], v7 offset:37888\n"
    "s_waitcnt vmcnt(9) lgkmcnt(3)\n"
    "v_mfma_f32_16x16x32_bf16 v[56:59], v[24:27], v[40:43], v[56:59]\n"
    "ds_read_b128 v[40:43], v7 offset:30720\n"
    "s_waitcnt lgkmcnt(3)\n"
    "v_mfma_f32_16x16x32_bf16 v[60:63], v[24:27], v[44:47], v[60:63]\n"
    "ds_read_b128 v[44:47], v7 offset:38912\n"
    "s_waitcnt vmcnt(8) lgkmcnt(3)\n"
    "v_mfma_f32_16x16x32_bf16 v[56:59], v[28:31], v[48:51], v[56:59]\n"
    "ds_read_b128 v[48:51], v7 offset:31744\n"
    "s_waitcnt lgkmcnt(3)\n"
    "v_mfma_f32_16x16x32_bf16 v[60:63], v[28:31], v[52:55], v[60:63]\n"
    "buffer_load_dwordx4 v[96:99], v4, s[40:43], s47 offen offset:2048 sc1\n"
    "ds_read_b128 v[52:55], v7 offset:39936\n"
    "s_waitcnt vmcnt(8) lgkmcnt(3)\n"
    "v_mfma_f32_16x16x32_bf16 v[56:59], v[32:35], v[40:43], v[56:59]\n"
    "ds_read_b128 v[40:43], v7 offset:40960\n"
    "s_waitcnt lgkmcnt(3)\n"
    "v_mfma_f32_16x16x32_bf16 v[60:63], v[32:35], v[44:47], v[60:63]\n"
    "ds_read_b128 v[44:47], v7 offset:49152\n"
    "s_waitcnt vmcnt(7) lgkmcnt(3)\n"
    "v_mfma_f32_16x16x32_bf16 v[56:59], v[36:39], v[48:51], v[56:59]\n"
    "ds_read_b128 v[48:51], v7 offset:41984\n"
    "s_waitcnt lgkmcnt(3)\n"
    "v_mfma_f32_16x16x32_bf16 v[60:63], v[36:39], v[52:55], v[60:63]\n"
    "ds_read_b128 v[52:55], v7 offset:50176\n"
    "s_waitcnt lgkmcnt(3)\n"
    "v_mfma_f32_16x16x32_bf16 v[64:67], v[8:11], v[40:43], 0\n"
    "buffer_load_dwordx4 v[100:103], v4, s[40:43], s47 offen offset:3072 sc1\n"
    "ds_read_b128 v[40:43], v7 offset:43008\n"
    "s_waitcnt lgkmcnt(3)\n"
    "v_mfma_f32_16x16x32_bf16 v[68:71], v[8:11], v[44:47], 0\n"
    "ds_read_b128 v[44:47], v7 offset:51200\n"
    "s_waitcnt lgkmcnt(3)\n"
    "v_mfma_f32_16x16x32_bf16 v[64:67], v[12:15], v[48:51], v[64:67]\n"
    "ds_read_b128 v[48:51], v7 offset:44032\n"
    "s_waitcnt lgkmcnt(3)\n"
    "v_mfma_f32_16x16x32_bf16 v[68:71], v[12:15], v[52:55], v[68:71]\n"
    "ds_read_b128 v[52:55], v7 offset:52224\n"
    "s_waitcnt lgkmcnt(3)\n"
    "v_mfma_f32_16x16x32_bf16 v[64:67], v[16:19], v[40:43], v[64:67]\n"
    "ds_read_b128 v[40:43], v7 offset:45056\n"
    "s_waitcnt lgkmcnt(3)\n"
    "v_mfma_f32_16x16x32_bf16 v[68:71], v[16:19], v[44:47], v[68:71]\n"
    "v_cvt_pk_bf16_f32 v56, v56, v57\n"
    "v_cvt_pk_bf16_f32 v57, v58, v59\n"
    "v_cvt_pk_bf16_f32 v58, v60, v61\n"
    "v_cvt_pk_bf16_f32 v59, v62, v63\n"
    "buffer_store_dwordx4 v[56:59], v0, s[40:43], s61 offen sc1\n"
    "ds_read_b128 v[44:47], v7 offset:53248\n"
    "s_waitcnt lgkmcnt(3)\n"
    "v_mfma_f32_16x16x32_bf16 v[64:67], v[20:23], v[48:51], v[64:67]\n"
    "ds_read_b128 v[48:51], v7 offset:46080\n"
    "s_waitcnt lgkmcnt(3)\n"
    "v_mfma_f32_16x16x32_bf16 v[68:71], v[20:23], v[52:55], v[68:71]\n"
    "ds_read_b128 v[52:55], v7 offset:54272\n"
    "s_waitcnt lgkmcnt(3)\n"
    "v_mfma_f32_16x16x32_bf16 v[64:67], v[24:27], v[40:43], v[64:67]\n"
    "ds_read_b128 v[40:43], v7 offset:47104\n"
    "s_waitcnt lgkmcnt(3)\n"
    "v_mfma_f32_16x16x32_bf16 v[68:71], v[24:27], v[44:47], v[68:71]\n"
    "ds_read_b128 v[44:47], v7 offset:55296\n"
    "s_waitcnt lgkmcnt(3)\n"
    "v_mfma_f32_16x16x32_bf16 v[64:67], v[28:31], v[48:51], v[64:67]\n"
    "ds_read_b128 v[48:51], v7 offset:48128\n"
    "s_waitcnt lgkmcnt(3)\n"
    "v_mfma_f32_16x16x32_bf16 v[68:71], v[28:31], v[52:55], v[68:71]\n"
    "ds_read_b128 v[52:55], v7 offset:56320\n"
    "s_waitcnt lgkmcnt(3)\n"
    "v_mfma_f32_16x16x32_bf16 v[64:67], v[32:35], v[40:43], v[64:67]\n"
    "s_waitcnt lgkmcnt(2)\n"
    "v_mfma_f32_16x16x32_bf16 v[68:71], v[32:35], v[44:47], v[68:71]\n"
    "s_waitcnt lgkmcnt(1)\n"
    "v_mfma_f32_16x16x32_bf16 v[64:67], v[36:39], v[48:51], v[64:67]\n"
    "s_waitcnt lgkmcnt(0)\n"
    "v_mfma_f32_16x16x32_bf16 v[68:71], v[36:39], v[52:55], v[68:71]\n"
    "v_and_b32 v5, 31, v1\n"
    "v_mul_u32_u24 v5, 768, v5\n"
    "v_lshrrev_b32 v6, 5, v1\n"
    "v_lshl_add_u32 v5, v6, 4, v5\n"
    "v_lshlrev_b32 v6, 3, v1\n"
    "s_nop 0\n"
    "v_cvt_pk_bf16_f32 v64, v64, v65\n"
    "v_cvt_pk_bf16_f32 v65, v66, v67\n"
    "v_cvt_pk_bf16_f32 v66, v68, v69\n"
    "v_cvt_pk_bf16_f32 v67, v70, v71\n"
    "buffer_store_dwordx4 v[64:67], v0, s[40:43], s56 offen sc1\n"
    "s_cmp_lg_u32 s3, 8\n"
    "s_cbranch_scc1 L_noml0\n"
    "v_lshlrev_b32 v8, 5, v1\n"
    "v_add_u32 v8, 106496, v8\n"
    "ds_read_b128 v[40:43], v8 offset:0\n"
    "ds_read_b128 v[44:47], v8 offset:16\n"
    "ds_read_b128 v[48:51], v8 offset:2048\n"
    "ds_read_b128 v[52:55], v8 offset:2064\n"
    "s_sub_u32 s56, s62, 65536\n"
    "s_lshr_b32 s57, s56, 11\n"
    "s_lshl_b32 s57, s57, 3\n"
    "s_bfe_u32 s56, s56, 0x30003\n"
    "s_or_b32 s56, s56, s57\n"
    "s_lshl_b32 s56, s56, 9\n"
    "s_add_u32 s59, s56, 393216\n"
    "s_waitcnt lgkmcnt(3)\n"
    "v_max3_f32 v40, v40, v41, v42\n"
    "s_waitcnt lgkmcnt(2)\n"
    "v_max3_f32 v43, v43, v44, v45\n"
    "v_max3_f32 v40, v40, v46, v47\n"
    "v_max_f32 v40, v40, v43\n"
    "v_mul_f32 v12, 0x3dd53b95, v40\n"
    "s_waitcnt lgkmcnt(1)\n"
    "v_pk_add_f32 v[48:49], v[48:49], v[50:51]\n"
    "s_waitcnt lgkmcnt(0)\n"
    "v_pk_add_f32 v[52:53], v[52:53], v[54:55]\n"
    "v_pk_add_f32 v[48:49], v[48:49], v[52:53]\n"
    "v_add_f32 v13, v48, v49\n"
    "buffer_store_dwordx2 v[12:13], v6, s[40:43], s59 offen sc1\n"
    "L_noml0:\n"
    "s_add_u32 s12, s97, 0\n"
    "s_add_u32 s13, s97, 16384\n"
    "s_add_u32 s14, s97, 32768\n"
    "s_add_u32 s15, s97, 49152\n"
    "s_add_u32 s16, s97, 65536\n"
    "s_add_u32 s17, s97, 81920\n"
    "s_add_u32 s18, s97, 98304\n"
    "s_add_u32 s19, s97, 114688\n"
    "s_add_u32 s20, s99, 0\n"
    "s_add_u32 s21, s99, 512\n"
    "s_add_u32 s22, s99, 1024\n"
    "s_add_u32 s23, s99, 1536\n"
    "s_waitcnt vmcnt(0)\n"
    "s_mov_b64 exec, 1\n"
    "buffer_store_dwordx2 v[2:3], off, s[40:43], s62 sc1\n"
    "s_mov_b64 exec, -1\n"
    "s_barrier\n"
    "buffer_load_dwordx2 v[48:49], v6, s[40:43], s96 offen sc1\n"
    "buffer_load_dwordx4 v[40:43], v5, s[8:11], s63 offen\n"
    "buffer_load_dwordx4 v[44:47], v5, s[8:11], s63 offen offset:32\n"
    "buffer_load_dwordx4 v[104:107], v4, s[40:43], s48 offen offset:0 sc1\n"
    "buffer_load_dwordx4 v[108:111], v4, s[40:43], s48 offen offset:1024 sc1\n"
    "buffer_load_dwordx4 v[112:115], v4, s[40:43], s48 offen offset:2048 sc1\n"
    "buffer_load_dwordx4 v[116:119], v4, s[40:43], s48 offen offset:3072 sc1\n"
    "s_lshl_b32 s57, s3, 9\n"
    "s_add_u32 s57, s57, 155648\n"
    "v_add_u32 v7, s57, v6\n"
    "s_waitcnt vmcnt(6)\n"
    "v_cmp_ne_u32 vcc, s44, v49\n"
    "s_cbranch_vccz L_modb_ok\n"
    "s_movk_i32 s46, 32767\n"
    "s_waitcnt vmcnt(0)\n"
    "L_modb:\n"
    "buffer_load_dwordx2 v[48:49], v6, s[40:43], s96 offen sc1\n"
    "s_sub_u32 s46, s46, 1\n"
    "s_waitcnt vmcnt(0)\n"
    "v_cmp_ne_u32 vcc, s44, v49\n"
    "s_cbranch_vccz L_modb_ok\n"
    "s_cbranch_scc0 L_modb\n"
    "L_modb_ok:\n"
    "v_and_b32 v49, 0xffff0000, v48\n"
    "v_lshlrev_b32 v48, 16, v48\n"
    "ds_write_b64 v7, v[48:49]\n"
    "v_and_b32 v5, 15, v1\n"
    "v_lshlrev_b32 v5, 4, v5\n"
    "v_bfe_u32 v7, v1, 4, 1\n"
    "v_lshl_add_u32 v7, v7, 9, v5\n"
    "v_lshrrev_b32 v54, 5, v1\n"
    "v_lshl_add_u32 v7, v54, 10, v7\n"
    "s_lshl_b32 s57, s3, 11\n"
    "s_add_u32 s57, s57, 49152\n"
    "v_add_u32 v7, s57, v7\n"
    "v_add_u32 v5, 106496, v5\n"
    "v_mul_u32_u24 v54, 1792, v54\n"
    "v_add_u32 v54, v6, v54\n"
    "v_and_b32 v6, 56, v6\n"
    "s_waitcnt vmcnt(5)\n"
    "v_permlane32_swap_b32 v40, v42\n"
    "s_waitcnt vmcnt(4)\n"
    "v_permlane32_swap_b32 v44, v46\n"
    "v_permlane32_swap_b32 v41, v43\n"
    "v_permlane32_swap_b32 v45, v47\n"
    "buffer_load_dwordx2 v[52:53], v6, s[40:43], s98 offen sc1\n"
    "s_movk_i32 s46, 32767\n"
    "L_fp_27:\n"
    "s_sub_u32 s46, s46, 1\n"
    "s_waitcnt vmcnt(0)\n"
    "v_cmp_ne_u64 vcc, s[44:45], v[52:53]\n"
    "s_cbranch_vccz L_fp_ok_28\n"
    "buffer_load_dwordx2 v[52:53], v6, s[40:43], s98 offen sc1\n"
    "s_cbranch_scc0 L_fp_27\n"
    "L_fp_ok_28:\n"
    "s_cmp_lg_u32 s3, 8\n"
    "s_cbranch_scc1 L_noml\n"
    "buffer_load_dwordx2 v[56:57], v54, s[40:43], s20 offen sc1\n"
    "buffer_load_dwordx2 v[58:59], v54, s[40:43], s21 offen sc1\n"
    "buffer_load_dwordx2 v[60:61], v54, s[40:43], s22 offen sc1\n"
    "buffer_load_dwordx2 v[62:63], v54, s[40:43], s23 offen sc1\n"
    "buffer_load_dwordx4 v[8:11], v0, s[40:43], s12 offen sc1\n"
    "buffer_load_dwordx4 v[12:15], v0, s[40:43], s13 offen sc1\n"
    "buffer_load_dwordx4 v[16:19], v0, s[40:43], s14 offen sc1\n"
    "buffer_load_dwordx4 v[20:23], v0, s[40:43], s15 offen sc1\n"
    "buffer_load_dwordx4 v[24:27], v0, s[40:43], s16 offen sc1\n"
    "buffer_load_dwordx4 v[28:31], v0, s[40:43], s17 offen sc1\n"
    "buffer_load_dwordx4 v[32:35], v0, s[40:43], s18 offen sc1\n"
    "buffer_load_dwordx4 v[36:39], v0, s[40:43], s19 offen sc1\n"
    "v_add_u32 v6, 106496, v0\n"
    "s_waitcnt vmcnt(9)\n"
    "v_max3_f32 v64, v56, v58, v60\n"
    "s_waitcnt vmcnt(8)\n"
    "v_max_f32 v64, v64, v62\n"
    "v_sub_f32 v68, v56, v64\n"
    "v_sub_f32 v69, v58, v64\n"
    "v_sub_f32 v70, v60, v64\n"
    "v_sub_f32 v71, v62, v64\n"
    "v_exp_f32 v68, v68\n"
    "v_exp_f32 v69, v69\n"
    "v_exp_f32 v70, v70\n"
    "v_exp_f32 v71, v71\n"
    "v_mul_f32 v64, v68, v57\n"
    "v_fmac_f32 v64, v69, v59\n"
    "v_fmac_f32 v64, v70, v61\n"
    "v_fmac_f32 v64, v71, v63\n"
    "v_rcp_f32 v64, v64\n"
    "s_nop 0\n"
    "v_mul_f32 v68, v68, v64\n"
    "v_mul_f32 v69, v69, v64\n"
    "v_mul_f32 v70, v70, v64\n"
    "v_mul_f32 v71, v71, v64\n"
    "ds_write_b128 v6, v[68:71]\n"
    "s_branch L_mlok\n"
    "L_noml:\n"
    "buffer_load_dwordx4 v[8:11], v0, s[40:43], s12 offen sc1\n"
    "buffer_load_dwordx4 v[12:15], v0, s[40:43], s13 offen sc1\n"
    "buffer_load_dwordx4 v[16:19], v0, s[40:43], s14 offen sc1\n"
    "buffer_load_dwordx4 v[20:23], v0, s[40:43], s15 offen sc1\n"
    "buffer_load_dwordx4 v[24:27], v0, s[40:43], s16 offen sc1\n"
    "buffer_load_dwordx4 v[28:31], v0, s[40:43], s17 offen sc1\n"
    "buffer_load_dwordx4 v[32:35], v0, s[40:43], s18 offen sc1\n"
    "buffer_load_dwordx4 v[36:39], v0, s[40:43], s19 offen sc1\n"
    "L_mlok:\n"
    "s_waitcnt lgkmcnt(0)\n"
    "s_barrier\n"
    "ds_read_b128 v[48:51], v5\n"
    "s_waitcnt vmcnt(8)\n"
    "ds_read_b128 v[52:55], v5 offset:256\n"
    "s_waitcnt vmcnt(7)\n"
    "v_lshlrev_b32 v66, 16, v8\n"
    "v_and_b32 v67, 0xffff0000, v8\n"
    "s_waitcnt lgkmcnt(1)\n"
    "v_pk_mul_f32 v[56:57], v[66:67], v[48:49] op_sel:[0,0] op_sel_hi:[1,0]\n"
    "v_lshlrev_b32 v70, 16, v9\n"
    "v_and_b32 v71, 0xffff0000, v9\n"
    "v_pk_mul_f32 v[58:59], v[70:71], v[48:49] op_sel:[0,0] op_sel_hi:[1,0]\n"
    "v_lshlrev_b32 v66, 16, v10\n"
    "v_and_b32 v67, 0xffff0000, v10\n"
    "s_waitcnt lgkmcnt(0)\n"
    "v_pk_mul_f32 v[60:61], v[66:67], v[52:53] op_sel:[0,0] op_sel_hi:[1,0]\n"
    "v_lshlrev_b32 v70, 16, v11\n"
    "v_and_b32 v71, 0xffff0000, v11\n"
    "v_pk_mul_f32 v[62:63], v[70:71], v[52:53] op_sel:[0,0] op_sel_hi:[1,0]\n"
    "buffer_load_dwordx4 v[120:123], v4, s[40:43], s49 offen offset:0 sc1\n"
    "buffer_load_dwordx4 v[124:127], v4, s[40:43], s49 offen offset:1024 sc1\n"
    "s_waitcnt vmcnt(8)\n"
    "v_lshlrev_b32 v66, 16, v12\n"
    "v_and_b32 v67, 0xffff0000, v12\n"
    "v_pk_fma_f32 v[56:57], v[66:67], v[48:49], v[56:57] op_sel:[0,1,0] op_sel_hi:[1,1,1]\n"
    "v_lshlrev_b32 v70, 16, v13\n"
    "v_and_b32 v71, 0xffff0000, v13\n"
    "v_pk_fma_f32 v[58:59], v[70:71], v[48:49], v[58:59] op_sel:[0,1,0] op_sel_hi:[1,1,1]\n"
    "v_lshlrev_b32 v66, 16, v14\n"
    "v_and_b32 v67, 0xffff0000, v14\n"
    "v_pk_fma_f32 v[60:61], v[66:67], v[52:53], v[60:61] op_sel:[0,1,0] op_sel_hi:[1,1,1]\n"
    "v_lshlrev_b32 v70, 16, v15\n"
    "v_and_b32 v71, 0xffff0000, v15\n"
    "v_pk_fma_f32 v[62:63], v[70:71], v[52:53], v[62:63] op_sel:[0,1,0] op_sel_hi:[1,1,1]\n"
    "ds_read_b128 v[8:11], v5 offset:512\n"
    "ds_read_b128 v[12:15], v5 offset:768\n"
    "buffer_load_dwordx4 v[128:131], v4, s[40:43], s49 offen offset:2048 sc1\n"
    "buffer_load_dwordx4 v[132:135], v4, s[40:43], s49 offen offset:3072 sc1\n"
    "s_waitcnt vmcnt(9)\n"
    "v_lshlrev_b32 v66, 16, v16\n"
    "v_and_b32 v67, 0xffff0000, v16\n"
    "v_pk_fma_f32 v[56:57], v[66:67], v[50:51], v[56:57] op_sel:[0,0,0] op_sel_hi:[1,0,1]\n"
    "v_lshlrev_b32 v70, 16, v17\n"
    "v_and_b32 v71, 0xffff0000, v17\n"
    "v_pk_fma_f32 v[58:59], v[70:71], v[50:51], v[58:59] op_sel:[0,0,0] op_sel_hi:[1,0,1]\n"
    "v_lshlrev_b32 v66, 16, v18\n"
    "v_and_b32 v67, 0xffff0000, v18\n"
    "v_pk_fma_f32 v[60:61], v[66:67], v[54:55], v[60:61] op_sel:[0,0,0] op_sel_hi:[1,0,1]\n"
    "v_lshlrev_b32 v70, 16, v19\n"
    "v_and_b32 v71, 0xffff0000, v19\n"
    "v_pk_fma_f32 v[62:63], v[70:71], v[54:55], v[62:63] op_sel:[0,0,0] op_sel_hi:[1,0,1]\n"
    "buffer_load_dwordx4 v[136:139], v4, s[40:43], s50 offen offset:0 sc1\n"
    "buffer_load_dwordx4 v[140:143], v4, s[40:43], s50 offen offset:1024 sc1\n"
    "s_waitcnt vmcnt(10)\n"
    "v_lshlrev_b32 v66, 16, v20\n"
    "v_and_b32 v67, 0xffff0000, v20\n"
    "v_pk_fma_f32 v[56:57], v[66:67], v[50:51], v[56:57] op_sel:[0,1,0] op_sel_hi:[1,1,1]\n"
    "v_lshlrev_b32 v70, 16, v21\n"
    "v_and_b32 v71, 0xffff0000, v21\n"
    "v_pk_fma_f32 v[58:59], v[70:71], v[50:51], v[58:59] op_sel:[0,1,0] op_sel_hi:[1,1,1]\n"
    "v_lshlrev_b32 v66, 16, v22\n"
    "v_and_b32 v67, 0xffff0000, v22\n"
    "v_pk_fma_f32 v[60:61], v[66:67], v[54:55], v[60:61] op_sel:[0,1,0] op_sel_hi:[1,1,1]\n"
    "v_lshlrev_b32 v70, 16, v23\n"
    "v_and_b32 v71, 0xffff0000, v23\n"
    "v_pk_fma_f32 v[62:63], v[70:71], v[54:55], v[62:63] op_sel:[0,1,0] op_sel_hi:[1,1,1]\n"
    "buffer_load_dwordx4 v[144:147], v4, s[40:43], s50 offen offset:2048 sc1\n"
    "buffer_load_dwordx4 v[148:151], v4, s[40:43], s50 offen offset:3072 sc1\n"
    "v_cvt_pk_bf16_f32 v64, v56, v57\n"
    "v_cvt_pk_bf16_f32 v65, v58, v59\n"
    "v_cvt_pk_bf16_f32 v68, v60, v61\n"
    "v_cvt_pk_bf16_f32 v69, v62, v63\n"
    "s_waitcnt vmcnt(11)\n"
    "v_lshlrev_b32 v16, 16, v24\n"
    "v_and_b32 v17, 0xffff0000, v24\n"
    "s_waitcnt lgkmcnt(1)\n"
    "v_pk_mul_f32 v[56:57], v[16:17], v[8:9] op_sel:[0,0] op_sel_hi:[1,0]\n"
    "v_lshlrev_b32 v18, 16, v25\n"
    "v_and_b32 v19, 0xffff0000, v25\n"
    "v_pk_mul_f32 v[58:59], v[18:19], v[8:9] op_sel:[0,0] op_sel_hi:[1,0]\n"
    "v_lshlrev_b32 v16, 16, v26\n"
    "v_and_b32 v17, 0xffff0000, v26\n"
    "s_waitcnt lgkmcnt(0)\n"
    "v_pk_mul_f32 v[60:61], v[16:17], v[12:13] op_sel:[0,0] op_sel_hi:[1,0]\n"
    "v_lshlrev_b32 v18, 16, v27\n"
    "v_and_b32 v19, 0xffff0000, v27\n"
    "v_pk_mul_f32 v[62:63], v[18:19], v[12:13] op_sel:[0,0] op_sel_hi:[1,0]\n"
    "buffer_load_dwordx4 v[152:155], v4, s[40:43], s51 offen offset:0 sc1\n"
    "buffer_load_dwordx4 v[156:159], v4, s[40:43], s51 offen offset:1024 sc1\n"
    "s_waitcnt vmcnt(12)\n"
    "v_lshlrev_b32 v16, 16, v28\n"
    "v_and_b32 v17, 0xffff0000, v28\n"
    "v_pk_fma_f32 v[56:57], v[16:17], v[8:9], v[56:57] op_sel:[0,1,0] op_sel_hi:[1,1,1]\n"
    "v_lshlrev_b32 v18, 16, v29\n"
    "v_and_b32 v19, 0xffff0000, v29\n"
    "v_pk_fma_f32 v[58:59], v[18:19], v[8:9], v[58:59] op_sel:[0,1,0] op_sel_hi:[1,1,1]\n"
    "v_lshlrev_b32 v16, 16, v30\n"
    "v_and_b32 v17, 0xffff0000, v30\n"
    "v_pk_fma_f32 v[60:61], v[16:17], v[12:13], v[60:61] op_sel:[0,1,0] op_sel_hi:[1,1,1]\n"
    "v_lshlrev_b32 v18, 16, v31\n"
    "v_and_b32 v19, 0xffff0000, v31\n"
    "v_pk_fma_f32 v[62:63], v[18:19], v[12:13], v[62:63] op_sel:[0,1,0] op_sel_hi:[1,1,1]\n"
    "buffer_load_dwordx4 v[160:163], v4, s[40:43], s51 offen offset:2048 sc1\n"
    "buffer_load_dwordx4 v[164:167], v4, s[40:43], s51 offen offset:3072 sc1\n"
    "s_waitcnt vmcnt(13)\n"
    "v_lshlrev_b32 v16, 16, v32\n"
    "v_and_b32 v17, 0xffff0000, v32\n"
    "v_pk_fma_f32 v[56:57], v[16:17], v[10:11], v[56:57] op_sel:[0,0,0] op_sel_hi:[1,0,1]\n"
    "v_lshlrev_b32 v18, 16, v33\n"
    "v_and_b32 v19, 0xffff0000, v33\n"
    "v_pk_fma_f32 v[58:59], v[18:19], v[10:11], v[58:59] op_sel:[0,0,0] op_sel_hi:[1,0,1]\n"
    "v_lshlrev_b32 v16, 16, v34\n"
    "v_and_b32 v17, 0xffff0000, v34\n"
    "v_pk_fma_f32 v[60:61], v[16:17], v[14:15], v[60:61] op_sel:[0,0,0] op_sel_hi:[1,0,1]\n"
    "v_lshlrev_b32 v18, 16, v35\n"
    "v_and_b32 v19, 0xffff0000, v35\n"
    "v_pk_fma_f32 v[62:63], v[18:19], v[14:15], v[62:63] op_sel:[0,0,0] op_sel_hi:[1,0,1]\n"
    "s_waitcnt vmcnt(12)\n"
    "v_lshlrev_b32 v16, 16, v36\n"
    "v_and_b32 v17, 0xffff0000, v36\n"
    "v_pk_fma_f32 v[56:57], v[16:17], v[10:11], v[56:57] op_sel:[0,1,0] op_sel_hi:[1,1,1]\n"
    "v_lshlrev_b32 v18, 16, v37\n"
    "v_and_b32 v19, 0xffff0000, v37\n"
    "v_pk_fma_f32 v[58:59], v[18:19], v[10:11], v[58:59] op_sel:[0,1,0] op_sel_hi:[1,1,1]\n"
    "v_lshlrev_b32 v16, 16, v38\n"
    "v_and_b32 v17, 0xffff0000, v38\n"
    "v_pk_fma_f32 v[60:61], v[16:17], v[14:15], v[60:61] op_sel:[0,1,0] op_sel_hi:[1,1,1]\n"
    "v_lshlrev_b32 v18, 16, v39\n"
    "v_and_b32 v19, 0xffff0000, v39\n"
    "v_pk_fma_f32 v[62:63], v[18:19], v[14:15], v[62:63] op_sel:[0,1,0] op_sel_hi:[1,1,1]\n"
    "v_cvt_pk_bf16_f32 v66, v56, v57\n"
    "v_cvt_pk_bf16_f32 v67, v58, v59\n"
    "v_cvt_pk_bf16_f32 v70, v60, v61\n"
    "v_cvt_pk_bf16_f32 v71, v62, v63\n"
    "ds_write_b128 v7, v[64:67]\n"
    "ds_write_b128 v7, v[68:71] offset:256\n"
    "s_mul_i32 s56, s6, 294912\n"
    "s_add_u32 s56, s56, 294912\n"
    "v_add_u32 v4, s56, v4\n"
    "v_add_u32 v6, 1179648, v4\n"
    "v_add_u32 v5, 49152, v0\n"
    "s_waitcnt lgkmcnt(0)\n"
    "s_barrier\n"
    "ds_read_b128 v[36:39], v5 offset:0\n"
    "ds_read_b128 v[48:51], v5 offset:1024\n"
    "ds_read_b128 v[52:55], v5 offset:2048\n"
    "s_waitcnt lgkmcnt(2)\n"
    "v_mfma_f32_32x32x16_bf16 v[56:71], v[72:75], v[36:39], 0\n"
    "ds_read_b128 v[36:39], v5 offset:3072\n"
    "s_waitcnt lgkmcnt(2)\n"
    "v_mfma_f32_32x32x16_bf16 v[56:71], v[76:79], v[48:51], v[56:71]\n"
    "ds_read_b128 v[48:51], v5 offset:4096\n"
    "s_waitcnt lgkmcnt(2)\n"
    "v_mfma_f32_32x32x16_bf16 v[56:71], v[80:83], v[52:55], v[56:71]\n"
    "buffer_load_dwordx4 v[8:11], v4, s[40:43], 0 offen offset:0 sc1\n"
    "ds_read_b128 v[52:55], v5 offset:5120\n"
    "s_waitcnt lgkmcnt(2)\n"
    "v_mfma_f32_32x32x16_bf16 v[56:71], v[84:87], v[36:39], v[56:71]\n"
    "ds_read_b128 v[36:39], v5 offset:6144\n"
    "s_waitcnt lgkmcnt(2)\n"
    "v_mfma_f32_32x32x16_bf16 v[56:71], v[88:91], v[48:51], v[56:71]\n"
    "buffer_load_dwordx4 v[12:15], v4, s[40:43], 0 offen offset:1024 sc1\n"
    "ds_read_b128 v[48:51], v5 offset:7168\n"
    "s_waitcnt lgkmcnt(2)\n"
    "v_mfma_f32_32x32x16_bf16 v[56:71], v[92:95], v[52:55], v[56:71]\n"
    "ds_read_b128 v[52:55], v5 offset:8192\n"
    "s_waitcnt lgkmcnt(2)\n"
    "v_mfma_f32_32x32x16_bf16 v[56:71], v[96:99], v[36:39], v[56:71]\n"
    "buffer_load_dwordx4 v[16:19], v4, s[40:43], 0 offen offset:2048 sc1\n"
    "ds_read_b128 v[36:39], v5 offset:9216\n"
    "s_waitcnt lgkmcnt(2)\n"
    "v_mfma_f32_32x32x16_bf16 v[56:71], v[100:103], v[48:51], v[56:71]\n"
    "ds_read_b128 v[48:51], v5 offset:10240\n"
    "s_waitcnt lgkmcnt(2)\n"
    "v_mfma_f32_32x32x16_bf16 v[56:71], v[104:107], v[52:55], v[56:71]\n"
    "buffer_load_dwordx4 v[20:23], v4, s[40:43], 0 offen offset:3072 sc1\n"
    "ds_read_b128 v[52:55], v5 offset:11264\n"
    "s_waitcnt lgkmcnt(2)\n"
    "v_mfma_f32_32x32x16_bf16 v[56:71], v[108:111], v[36:39], v[56:71]\n"
    "ds_read_b128 v[36:39], v5 offset:12288\n"
    "s_waitcnt lgkmcnt(2)\n"
    "v_mfma_f32_32x32x16_bf16 v[56:71], v[112:115], v[48:51], v[56:71]\n"
    "buffer_load_dwordx4 v[24:27], v4, s[40:43], s47 offen offset:0 sc1\n"
    "ds_read_b128 v[48:51], v5 offset:13312\n"
    "s_waitcnt lgkmcnt(2)\n"
    "v_mfma_f32_32x32x16_bf16 v[56:71], v[116:119], v[52:55], v[56:71]\n"
    "ds_read_b128 v[52:55], v5 offset:14336\n"
    "s_waitcnt vmcnt(16) lgkmcnt(2)\n"
    "v_mfma_f32_32x32x16_bf16 v[56:71], v[120:123], v[36:39], v[56:71]\n"
    "v_lshrrev_b32 v122, 5, v1\n"
    "s_lshl_b32 s56, s3, 7\n"
    "s_add_u32 s56, s56, 155648\n"
    "v_lshl_add_u32 v120, v122, 4, s56\n"
    "ds_read_b128 v[36:39], v5 offset:15360\n"
    "s_waitcnt vmcnt(15) lgkmcnt(2)\n"
    "v_mfma_f32_32x32x16_bf16 v[56:71], v[124:127], v[48:51], v[56:71]\n"
    "buffer_load_dwordx4 v[28:31], v4, s[40:43], s47 offen offset:1024 sc1\n"
    "v_and_b32 v121, 31, v1\n"
    "v_lshlrev_b32 v121, 3, v121\n"
    "v_add_u32 v121, 107520, v121\n"
    "ds_read_b128 v[48:51], v5 offset:16384\n"
    "s_waitcnt vmcnt(15) lgkmcnt(2)\n"
    "v_mfma_f32_32x32x16_bf16 v[56:71], v[128:131], v[52:55], v[56:71]\n"
    "s_lshl_b32 s56, s3, 8\n"
    "v_lshl_add_u32 v123, v122, 2, s56\n"
    "v_add_u32 v123, v123, v121\n"
    "v_mul_u32_u24 v122, 1536, v122\n"
    "v_add_u32 v121, v122, v121\n"
    "ds_read_b128 v[52:55], v5 offset:17408\n"
    "s_waitcnt vmcnt(14) lgkmcnt(2)\n"
    "v_mfma_f32_32x32x16_bf16 v[56:71], v[132:135], v[36:39], v[56:71]\n"
    "buffer_load_dwordx4 v[32:35], v4, s[40:43], s47 offen offset:2048 sc1\n"
    "ds_read_b128 v[36:39], v5 offset:18432\n"
    "s_waitcnt vmcnt(14) lgkmcnt(2)\n"
    "v_mfma_f32_32x32x16_bf16 v[56:71], v[136:139], v[48:51], v[56:71]\n"
    "v_lshlrev_b32 v136, 16, v40\n"
    "v_and_b32 v137, 0xffff0000, v40\n"
    "v_lshlrev_b32 v138, 16, v41\n"
    "v_and_b32 v139, 0xffff0000, v41\n"
    "ds_read_b128 v[48:51], v5 offset:19456\n"
    "s_waitcnt vmcnt(13) lgkmcnt(2)\n"
    "v_mfma_f32_32x32x16_bf16 v[56:71], v[140:143], v[52:55], v[56:71]\n"
    "buffer_load_dwordx4 v[72:75], v4, s[40:43], s47 offen offset:3072 sc1\n"
    "v_lshlrev_b32 v132, 16, v42\n"
    "v_and_b32 v133, 0xffff0000, v42\n"
    "v_lshlrev_b32 v134, 16, v43\n"
    "v_and_b32 v135, 0xffff0000, v43\n"
    "ds_read_b128 v[52:55], v5 offset:20480\n"
    "s_waitcnt vmcnt(13) lgkmcnt(2)\n"
    "v_mfma_f32_32x32x16_bf16 v[56:71], v[144:147], v[36:39], v[56:71]\n"
    "v_lshlrev_b32 v128, 16, v44\n"
    "v_and_b32 v129, 0xffff0000, v44\n"
    "v_lshlrev_b32 v130, 16, v45\n"
    "v_and_b32 v131, 0xffff0000, v45\n"
    "ds_read_b128 v[36:39], v5 offset:21504\n"
    "s_waitcnt vmcnt(12) lgkmcnt(2)\n"
    "v_mfma_f32_32x32x16_bf16 v[56:71], v[148:151], v[48:51], v[56:71]\n"
    "buffer_load_dwordx4 v[76:79], v4, s[40:43], s48 offen offset:0 sc1\n"
    "v_lshlrev_b32 v124, 16, v46\n"
    "v_and_b32 v125, 0xffff0000, v46\n"
    "v_lshlrev_b32 v126, 16, v47\n"
    "v_and_b32 v127, 0xffff0000, v47\n"
    "ds_read_b128 v[48:51], v5 offset:22528\n"
    "s_waitcnt vmcnt(12) lgkmcnt(2)\n"
    "v_mfma_f32_32x32x16_bf16 v[56:71], v[152:155], v[52:55], v[56:71]\n"
    "ds_read_b128 v[52:55], v5 offset:23552\n"
    "s_waitcnt vmcnt(11) lgkmcnt(2)\n"
    "v_mfma_f32_32x32x16_bf16 v[56:71], v[156:159], v[36:39], v[56:71]\n"
    "buffer_load_dwordx4 v[80:83], v4, s[40:43], s48 offen offset:1024 sc1\n"
    "ds_read_b128 v[156:159], v120 offset:0\n"
    "ds_read_b128 v[152:155], v120 offset:32\n"
    "ds_read_b128 v[148:151], v120 offset:64\n"
    "ds_read_b128 v[144:147], v120 offset:96\n"
    "s_waitcnt vmcnt(11) lgkmcnt(5)\n"
    "v_mfma_f32_32x32x16_bf16 v[56:71], v[160:163], v[48:51], v[56:71]\n"
    "s_waitcnt vmcnt(10) lgkmcnt(4)\n"
    "v_mfma_f32_32x32x16_bf16 v[56:71], v[164:167], v[52:55], v[56:71]\n"
    "buffer_load_dwordx4 v[84:87], v4, s[40:43], s48 offen offset:2048 sc1\n"
    "s_waitcnt lgkmcnt(3)\n"
    "s_nop 9\n"
    "v_fma_f32 v56, v156, v56, v136\n"
    "v_fma_f32 v57, v157, v57, v137\n"
    "v_add_f32 v36, v56, v57\n"
    "v_mul_f32 v37, v56, v56\n"
    "v_fmac_f32 v37, v57, v57\n"
    "v_fma_f32 v58, v158, v58, v138\n"
    "v_add_f32 v36, v36, v58\n"
    "v_fmac_f32 v37, v58, v58\n"
    "v_fma_f32 v59, v159, v59, v139\n"
    "v_add_f32 v36, v36, v59\n"
    "v_fmac_f32 v37, v59, v59\n"
    "s_waitcnt lgkmcnt(2)\n"
    "v_fma_f32 v60, v152, v60, v132\n"
    "v_add_f32 v36, v36, v60\n"
    "v_fmac_f32 v37, v60, v60\n"
    "v_fma_f32 v61, v153, v61, v133\n"
    "v_add_f32 v36, v36, v61\n"
    "v_fmac_f32 v37, v61, v61\n"
    "v_fma_f32 v62, v154, v62, v134\n"
    "v_add_f32 v36, v36, v62\n"
    "v_fmac_f32 v37, v62, v62\n"
    "v_fma_f32 v63, v155, v63, v135\n"
    "v_add_f32 v36, v36, v63\n"
    "v_fmac_f32 v37, v63, v63\n"
    "s_waitcnt lgkmcnt(1)\n"
    "v_fma_f32 v64, v148, v64, v128\n"
    "v_add_f32 v36, v36, v64\n"
    "v_fmac_f32 v37, v64, v64\n"
    "v_fma_f32 v65, v149, v65, v129\n"
    "v_add_f32 v36, v36, v65\n"
    "v_fmac_f32 v37, v65, v65\n"
    "v_fma_f32 v66, v150, v66, v130\n"
    "v_add_f32 v36, v36, v66\n"
    "v_fmac_f32 v37, v66, v66\n"
    "v_fma_f32 v67, v151, v67, v131\n"
    "v_add_f32 v36, v36, v67\n"
    "v_fmac_f32 v37, v67, v67\n"
    "s_waitcnt lgkmcnt(0)\n"
    "v_fma_f32 v68, v144, v68, v124\n"
    "v_add_f32 v36, v36, v68\n"
    "v_fmac_f32 v37, v68, v68\n"
    "v_fma_f32 v69, v145, v69, v125\n"
    "v_add_f32 v36, v36, v69\n"
    "v_fmac_f32 v37, v69, v69\n"
    "v_fma_f32 v70, v146, v70, v126\n"
    "v_add_f32 v36, v36, v70\n"
    "v_fmac_f32 v37, v70, v70\n"
    "v_fma_f32 v71, v147, v71, v127\n"
    "v_add_f32 v36, v36, v71\n"
    "v_fmac_f32 v37, v71, v71\n"
    "s_nop 1\n"
    "v_permlane32_swap_b32 v36, v37\n"
    "v_add_f32 v36, v36, v37\n"
    "ds_write_b32 v123, v36\n"
    "ds_read_b128 v[156:159], v120 offset:1536\n"
    "ds_read_b128 v[136:139], v120 offset:3072\n"
    "ds_read_b128 v[152:155], v120 offset:1568\n"
    "ds_read_b128 v[132:135], v120 offset:3104\n"
    "ds_read_b128 v[148:151], v120 offset:1600\n"
    "ds_read_b128 v[128:131], v120 offset:3136\n"
    "ds_read_b128 v[144:147], v120 offset:1632\n"
    "ds_read_b128 v[124:127], v120 offset:3168\n"
    "v_cvt_pk_bf16_f32 v40, v56, v57\n"
    "v_cvt_pk_bf16_f32 v41, v58, v59\n"
    "v_cvt_pk_bf16_f32 v42, v60, v61\n"
    "v_cvt_pk_bf16_f32 v43, v62, v63\n"
    "v_cvt_pk_bf16_f32 v44, v64, v65\n"
    "v_cvt_pk_bf16_f32 v45, v66, v67\n"
    "v_cvt_pk_bf16_f32 v46, v68, v69\n"
    "v_cvt_pk_bf16_f32 v47, v70, v71\n"
    "s_lshl_b32 s56, s3, 11\n"
    "s_add_u32 s56, s56, 73728\n"
    "v_add_u32 v7, s56, v0\n"
    "s_waitcnt lgkmcnt(0)\n"
    "s_barrier\n"
    "ds_read_b64 v[36:37], v121 offset:0\n"
    "ds_read_b64 v[38:39], v121 offset:256\n"
    "ds_read_b64 v[48:49], v121 offset:512\n"
    "ds_read_b64 v[50:51], v121 offset:768\n"
    "ds_read_b64 v[52:53], v121 offset:1024\n"
    "ds_read_b64 v[54:55], v121 offset:1280\n"
    "s_waitcnt lgkmcnt(4)\n"
    "v_pk_add_f32 v[36:37], v[36:37], v[38:39]\n"
    "s_waitcnt lgkmcnt(2)\n"
    "v_pk_add_f32 v[48:49], v[48:49], v[50:51]\n"
    "s_waitcnt lgkmcnt(0)\n"
    "v_pk_add_f32 v[52:53], v[52:53], v[54:55]\n"
    "v_pk_add_f32 v[36:37], v[36:37], v[48:49]\n"
    "v_pk_add_f32 v[36:37], v[36:37], v[52:53]\n"
    "v_mov_b32 v38, v36\n"
    "v_mov_b32 v39, v37\n"
    "s_nop 0\n"
    "v_permlane32_swap_b32 v36, v38\n"
    "v_permlane32_swap_b32 v37, v39\n"
    "v_pk_add_f32 v[36:37], v[36:37], v[38:39]\n"
    "v_mul_f32 v36, 0x3b2aaaab, v36\n"
    "v_mul_f32 v37, 0x3b2aaaab, v37\n"
    "v_fma_f32 v37, -v36, v36, v37\n"
    "v_max_f32 v37, 0, v37\n"
    "v_add_f32 v37, 0x3727c5ac, v37\n"
    "v_rsq_f32 v48, v37\n"
    "s_nop 0\n"
    "v_mul_f32 v50, v36, v48\n"
    "v_pk_fma_f32 v[56:57], v[56:57], v[48:49], v[50:51] op_sel_hi:[1,0,0] neg_lo:[0,0,1] neg_hi:[0,0,1]\n"
    "v_pk_fma_f32 v[56:57], v[56:57], v[136:137], v[156:157]\n"
    "v_pk_fma_f32 v[58:59], v[58:59], v[48:49], v[50:51] op_sel_hi:[1,0,0] neg_lo:[0,0,1] neg_hi:[0,0,1]\n"
    "v_pk_fma_f32 v[58:59], v[58:59], v[138:139], v[158:159]\n"
    "v_pk_fma_f32 v[60:61], v[60:61], v[48:49], v[50:51] op_sel_hi:[1,0,0] neg_lo:[0,0,1] neg_hi:[0,0,1]\n"
    "v_pk_fma_f32 v[60:61], v[60:61], v[132:133], v[152:153]\n"
    "v_pk_fma_f32 v[62:63], v[62:63], v[48:49], v[50:51] op_sel_hi:[1,0,0] neg_lo:[0,0,1] neg_hi:[0,0,1]\n"
    "v_pk_fma_f32 v[62:63], v[62:63], v[134:135], v[154:155]\n"
    "v_cvt_pk_bf16_f32 v156, v56, v57\n"
    "v_cvt_pk_bf16_f32 v157, v58, v59\n"
    "v_cvt_pk_bf16_f32 v158, v60, v61\n"
    "v_cvt_pk_bf16_f32 v159, v62, v63\n"
    "ds_write_b128 v7, v[156:159] offset:0\n"
    "v_pk_fma_f32 v[64:65], v[64:65], v[48:49], v[50:51] op_sel_hi:[1,0,0] neg_lo:[0,0,1] neg_hi:[0,0,1]\n"
    "v_pk_fma_f32 v[64:65], v[64:65], v[128:129], v[148:149]\n"
    "v_pk_fma_f32 v[66:67], v[66:67], v[48:49], v[50:51] op_sel_hi:[1,0,0] neg_lo:[0,0,1] neg_hi:[0,0,1]\n"
    "v_pk_fma_f32 v[66:67], v[66:67], v[130:131], v[150:151]\n"
    "v_pk_fma_f32 v[68:69], v[68:69], v[48:49], v[50:51] op_sel_hi:[1,0,0] neg_lo:[0,0,1] neg_hi:[0,0,1]\n"
    "v_pk_fma_f32 v[68:69], v[68:69], v[124:125], v[144:145]\n"
    "v_pk_fma_f32 v[70:71], v[70:71], v[48:49], v[50:51] op_sel_hi:[1,0,0] neg_lo:[0,0,1] neg_hi:[0,0,1]\n"
    "v_pk_fma_f32 v[70:71], v[70:71], v[126:127], v[146:147]\n"
    "v_cvt_pk_bf16_f32 v136, v64, v65\n"
    "v_cvt_pk_bf16_f32 v137, v66, v67\n"
    "v_cvt_pk_bf16_f32 v138, v68, v69\n"
    "v_cvt_pk_bf16_f32 v139, v70, v71\n"
    "ds_write_b128 v7, v[136:139] offset:1024\n"
    "buffer_load_dwordx4 v[88:91], v4, s[40:43], s48 offen offset:3072 sc1\n"
    "buffer_load_dwordx4 v[92:95], v4, s[40:43], s49 offen offset:0 sc1\n"
    "buffer_load_dwordx4 v[96:99], v4, s[40:43], s49 offen offset:1024 sc1\n"
    "s_waitcnt lgkmcnt(0)\n"
    "s_barrier\n"
    "ds_read_b128 v[36:39], v5 offset:24576\n"
    "ds_read_b128 v[48:51], v5 offset:25600\n"
    "ds_read_b128 v[52:55], v5 offset:26624\n"
    "s_waitcnt vmcnt(13) lgkmcnt(2)\n"
    "v_mfma_f32_32x32x16_bf16 v[56:71], v[8:11], v[36:39], 0\n"
    "ds_read_b128 v[36:39], v5 offset:27648\n"
    "s_waitcnt vmcnt(12) lgkmcnt(2)\n"
    "v_mfma_f32_32x32x16_bf16 v[56:71], v[12:15], v[48:51], v[56:71]\n"
    "ds_read_b128 v[48:51], v5 offset:28672\n"
    "s_waitcnt vmcnt(11) lgkmcnt(2)\n"
    "v_mfma_f32_32x32x16_bf16 v[56:71], v[16:19], v[52:55], v[56:71]\n"
    "buffer_load_dwordx4 v[100:103], v4, s[40:43], s49 offen offset:2048 sc1\n"
    "ds_read_b128 v[52:55], v5 offset:29696\n"
    "s_waitcnt vmcnt(11) lgkmcnt(2)\n"
    "v_mfma_f32_32x32x16_bf16 v[56:71], v[20:23], v[36:39], v[56:71]\n"
    "ds_read_b128 v[36:39], v5 offset:30720\n"
    "s_waitcnt vmcnt(10) lgkmcnt(2)\n"
    "v_mfma_f32_32x32x16_bf16 v[56:71], v[24:27], v[48:51], v[56:71]\n"
    "buffer_load_dwordx4 v[104:107], v4, s[40:43], s49 offen offset:3072 sc1\n"
    "ds_read_b128 v[48:51], v5 offset:31744\n"
    "s_waitcnt vmcnt(10) lgkmcnt(2)\n"
    "v_mfma_f32_32x32x16_bf16 v[56:71], v[28:31], v[52:55], v[56:71]\n"
    "ds_read_b128 v[52:55], v5 offset:32768\n"
    "s_waitcnt vmcnt(9) lgkmcnt(2)\n"
    "v_mfma_f32_32x32x16_bf16 v[56:71], v[32:35], v[36:39], v[56:71]\n"
    "buffer_load_dwordx4 v[108:111], v4, s[40:43], s50 offen offset:0 sc1\n"
    "ds_read_b128 v[36:39], v5 offset:33792\n"
    "s_waitcnt vmcnt(9) lgkmcnt(2)\n"
    "v_mfma_f32_32x32x16_bf16 v[56:71], v[72:75], v[48:51], v[56:71]\n"
    "ds_read_b128 v[48:51], v5 offset:34816\n"
    "s_waitcnt vmcnt(8) lgkmcnt(2)\n"
    "v_mfma_f32_32x32x16_bf16 v[56:71], v[76:79], v[52:55], v[56:71]\n"
    "buffer_load_dwordx4 v[112:115], v4, s[40:43], s50 offen offset:1024 sc1\n"
    "ds_read_b128 v[52:55], v5 offset:35840\n"
    "s_waitcnt vmcnt(8) lgkmcnt(2)\n"
    "v_mfma_f32_32x32x16_bf16 v[56:71], v[80:83], v[36:39], v[56:71]\n"
    "ds_read_b128 v[36:39], v5 offset:36864\n"
    "s_waitcnt vmcnt(7) lgkmcnt(2)\n"
    "v_mfma_f32_32x32x16_bf16 v[56:71], v[84:87], v[48:51], v[56:71]\n"
    "buffer_load_dwordx4 v[116:119], v4, s[40:43], s50 offen offset:2048 sc1\n"
    "ds_read_b128 v[48:51], v5 offset:37888\n"
    "s_waitcnt vmcnt(7) lgkmcnt(2)\n"
    "v_mfma_f32_32x32x16_bf16 v[56:71], v[88:91], v[52:55], v[56:71]\n"
    "ds_read_b128 v[52:55], v5 offset:38912\n"
    "s_waitcnt vmcnt(6) lgkmcnt(2)\n"
    "v_mfma_f32_32x32x16_bf16 v[56:71], v[92:95], v[36:39], v[56:71]\n"
    "ds_read_b128 v[36:39], v5 offset:39936\n"
    "s_waitcnt vmcnt(5) lgkmcnt(2)\n"
    "v_mfma_f32_32x32x16_bf16 v[56:71], v[96:99], v[48:51], v[56:71]\n"
    "buffer_load_dwordx4 v[140:143], v4, s[40:43], s50 offen offset:3072 sc1\n"
    "ds_read_b128 v[48:51], v5 offset:40960\n"
    "s_waitcnt vmcnt(5) lgkmcnt(2)\n"
    "v_mfma_f32_32x32x16_bf16 v[56:71], v[100:103], v[52:55], v[56:71]\n"
    "ds_read_b128 v[52:55], v5 offset:41984\n"
    "s_waitcnt vmcnt(4) lgkmcnt(2)\n"
    "v_mfma_f32_32x32x16_bf16 v[56:71], v[104:107], v[36:39], v[56:71]\n"
    "buffer_load_dwordx4 v[160:163], v4, s[40:43], s51 offen offset:0 sc1\n"
    "ds_read_b128 v[36:39], v5 offset:43008\n"
    "s_waitcnt vmcnt(4) lgkmcnt(2)\n"
    "v_mfma_f32_32x32x16_bf16 v[56:71], v[108:111], v[48:51], v[56:71]\n"
    "ds_read_b128 v[48:51], v5 offset:44032\n"
    "s_waitcnt vmcnt(3) lgkmcnt(2)\n"
    "v_mfma_f32_32x32x16_bf16 v[56:71], v[112:115], v[52:55], v[56:71]\n"
    "buffer_load_dwordx4 v[164:167], v4, s[40:43], s51 offen offset:1024 sc1\n"
    "ds_read_b128 v[52:55], v5 offset:45056\n"
    "s_waitcnt vmcnt(3) lgkmcnt(2)\n"
    "v_mfma_f32_32x32x16_bf16 v[56:71], v[116:119], v[36:39], v[56:71]\n"
    "ds_read_b128 v[36:39], v5 offset:46080\n"
    "s_waitcnt vmcnt(2) lgkmcnt(2)\n"
    "v_mfma_f32_32x32x16_bf16 v[56:71], v[140:143], v[48:51], v[56:71]\n"
    "buffer_load_dwordx4 v[156:159], v4, s[40:43], s51 offen offset:2048 sc1\n"
    "ds_read_b128 v[48:51], v5 offset:47104\n"
    "s_waitcnt vmcnt(2) lgkmcnt(2)\n"
    "v_mfma_f32_32x32x16_bf16 v[56:71], v[160:163], v[52:55], v[56:71]\n"
    "s_mov_b32 s56, 0xbdd2d3e8\n"
    "v_mov_b32 v122, 0xc0135761\n"
    "s_lshl_b32 s58, s3, 11\n"
    "s_add_u32 s58, s58, 49152\n"
    "v_add_u32 v7, s58, v0\n"
    "ds_read_b128 v[52:55], v5 offset:48128\n"
    "s_waitcnt vmcnt(1) lgkmcnt(2)\n"
    "v_mfma_f32_32x32x16_bf16 v[56:71], v[164:167], v[36:39], v[56:71]\n"
    "buffer_load_dwordx4 v[152:155], v4, s[40:43], s51 offen offset:3072 sc1\n"
    "s_waitcnt vmcnt(1) lgkmcnt(1)\n"
    "v_mfma_f32_32x32x16_bf16 v[56:71], v[156:159], v[48:51], v[56:71]\n"
    "s_waitcnt vmcnt(0) lgkmcnt(0)\n"
    "v_mfma_f32_32x32x16_bf16 v[56:71], v[152:155], v[52:55], v[56:71]\n"
    "buffer_load_dwordx4 v[148:151], v6, s[40:43], 0 offen offset:0 sc1\n"
    "s_nop 10\n"
    "v_mul_f32 v36, v56, v56\n"
    "v_mul_f32 v37, v57, v57\n"
    "v_mul_f32 v38, v58, v58\n"
    "v_mul_f32 v39, v59, v59\n"
    "v_mul_f32 v48, v60, v60\n"
    "v_mul_f32 v49, v61, v61\n"
    "v_mul_f32 v50, v62, v62\n"
    "v_mul_f32 v51, v63, v63\n"
    "v_fma_f32 v36, v36, s56, v122\n"
    "v_fma_f32 v37, v37, s56, v122\n"
    "v_fma_f32 v38, v38, s56, v122\n"
    "v_fma_f32 v39, v39, s56, v122\n"
    "v_fma_f32 v48, v48, s56, v122\n"
    "v_fma_f32 v49, v49, s56, v122\n"
    "v_fma_f32 v50, v50, s56, v122\n"
    "v_fma_f32 v51, v51, s56, v122\n"
    "v_mul_f32 v36, v36, v56\n"
    "v_mul_f32 v37, v37, v57\n"
    "v_mul_f32 v38, v38, v58\n"
    "v_mul_f32 v39, v39, v59\n"
    "v_mul_f32 v48, v48, v60\n"
    "v_mul_f32 v49, v49, v61\n"
    "v_mul_f32 v50, v50, v62\n"
    "v_mul_f32 v51, v51, v63\n"
    "v_exp_f32 v36, v36\n"
    "v_exp_f32 v37, v37\n"
    "v_exp_f32 v38, v38\n"
    "v_exp_f32 v39, v39\n"
    "v_exp_f32 v48, v48\n"
    "v_exp_f32 v49, v49\n"
    "v_exp_f32 v50, v50\n"
    "v_exp_f32 v51, v51\n"
    "v_add_f32 v36, 1.0, v36\n"
    "v_add_f32 v37, 1.0, v37\n"
    "v_add_f32 v38, 1.0, v38\n"
    "v_add_f32 v39, 1.0, v39\n"
    "v_add_f32 v48, 1.0, v48\n"
    "v_add_f32 v49, 1.0, v49\n"
    "v_add_f32 v50, 1.0, v50\n"
    "v_add_f32 v51, 1.0, v51\n"
    "buffer_load_dwordx4 v[144:147], v6, s[40:43], 0 offen offset:1024 sc1\n"
    "buffer_load_dwordx4 v[136:139], v6, s[40:43], 0 offen offset:2048 sc1\n"
    "v_rcp_f32 v36, v36\n"
    "v_rcp_f32 v37, v37\n"
    "v_rcp_f32 v38, v38\n"
    "v_rcp_f32 v39, v39\n"
    "v_rcp_f32 v48, v48\n"
    "v_rcp_f32 v49, v49\n"
    "v_rcp_f32 v50, v50\n"
    "v_rcp_f32 v51, v51\n"
    "v_mul_f32 v56, v56, v36\n"
    "v_mul_f32 v57, v57, v37\n"
    "v_mul_f32 v58, v58, v38\n"
    "v_mul_f32 v59, v59, v39\n"
    "v_mul_f32 v60, v60, v48\n"
    "v_mul_f32 v61, v61, v49\n"
    "v_mul_f32 v62, v62, v50\n"
    "v_mul_f32 v63, v63, v51\n"
    "v_cvt_pk_bf16_f32 v152, v56, v57\n"
    "v_cvt_pk_bf16_f32 v153, v58, v59\n"
    "v_cvt_pk_bf16_f32 v154, v60, v61\n"
    "v_cvt_pk_bf16_f32 v155, v62, v63\n"
    "ds_write_b128 v7, v[152:155] offset:0\n"
    "v_mul_f32 v36, v64, v64\n"
    "v_mul_f32 v37, v65, v65\n"
    "v_mul_f32 v38, v66, v66\n"
    "v_mul_f32 v39, v67, v67\n"
    "v_mul_f32 v48, v68, v68\n"
    "v_mul_f32 v49, v69, v69\n"
    "v_mul_f32 v50, v70, v70\n"
    "v_mul_f32 v51, v71, v71\n"
    "v_fma_f32 v36, v36, s56, v122\n"
    "v_fma_f32 v37, v37, s56, v122\n"
    "v_fma_f32 v38, v38, s56, v122\n"
    "v_fma_f32 v39, v39, s56, v122\n"
    "v_fma_f32 v48, v48, s56, v122\n"
    "v_fma_f32 v49, v49, s56, v122\n"
    "v_fma_f32 v50, v50, s56, v122\n"
    "v_fma_f32 v51, v51, s56, v122\n"
    "v_mul_f32 v36, v36, v64\n"
    "v_mul_f32 v37, v37, v65\n"
    "v_mul_f32 v38, v38, v66\n"
    "v_mul_f32 v39, v39, v67\n"
    "v_mul_f32 v48, v48, v68\n"
    "v_mul_f32 v49, v49, v69\n"
    "v_mul_f32 v50, v50, v70\n"
    "v_mul_f32 v51, v51, v71\n"
    "v_exp_f32 v36, v36\n"
    "v_exp_f32 v37, v37\n"
    "v_exp_f32 v38, v38\n"
    "v_exp_f32 v39, v39\n"
    "v_exp_f32 v48, v48\n"
    "v_exp_f32 v49, v49\n"
    "v_exp_f32 v50, v50\n"
    "v_exp_f32 v51, v51\n"
    "v_add_f32 v36, 1.0, v36\n"
    "v_add_f32 v37, 1.0, v37\n"
    "v_add_f32 v38, 1.0, v38\n"
    "v_add_f32 v39, 1.0, v39\n"
    "v_add_f32 v48, 1.0, v48\n"
    "v_add_f32 v49, 1.0, v49\n"
    "v_add_f32 v50, 1.0, v50\n"
    "v_add_f32 v51, 1.0, v51\n"
    "buffer_load_dwordx4 v[132:135], v6, s[40:43], 0 offen offset:3072 sc1\n"
    "buffer_load_dwordx4 v[128:131], v6, s[40:43], s47 offen offset:0 sc1\n"
    "v_rcp_f32 v36, v36\n"
    "v_rcp_f32 v37, v37\n"
    "v_rcp_f32 v38, v38\n"
    "v_rcp_f32 v39, v39\n"
    "v_rcp_f32 v48, v48\n"
    "v_rcp_f32 v49, v49\n"
    "v_rcp_f32 v50, v50\n"
    "v_rcp_f32 v51, v51\n"
    "v_mul_f32 v64, v64, v36\n"
    "v_mul_f32 v65, v65, v37\n"
    "v_mul_f32 v66, v66, v38\n"
    "v_mul_f32 v67, v67, v39\n"
    "v_mul_f32 v68, v68, v48\n"
    "v_mul_f32 v69, v69, v49\n"
    "v_mul_f32 v70, v70, v50\n"
    "v_mul_f32 v71, v71, v51\n"
    "v_cvt_pk_bf16_f32 v156, v64, v65\n"
    "v_cvt_pk_bf16_f32 v157, v66, v67\n"
    "v_cvt_pk_bf16_f32 v158, v68, v69\n"
    "v_cvt_pk_bf16_f32 v159, v70, v71\n"
    "ds_write_b128 v7, v[156:159] offset:1024\n"
    "buffer_load_dwordx4 v[124:127], v6, s[40:43], s47 offen offset:1024 sc1\n"
    "buffer_load_dwordx4 v[8:11], v6, s[40:43], s47 offen offset:2048 sc1\n"
    "buffer_load_dwordx4 v[12:15], v6, s[40:43], s47 offen offset:3072 sc1\n"
    "buffer_load_dwordx4 v[16:19], v6, s[40:43], s48 offen offset:0 sc1\n"
    "buffer_load_dwordx4 v[20:23], v6, s[40:43], s48 offen offset:1024 sc1\n"
    "buffer_load_dwordx4 v[24:27], v6, s[40:43], s48 offen offset:2048 sc1\n"
    "buffer_load_dwordx4 v[28:31], v6, s[40:43], s48 offen offset:3072 sc1\n"
    "buffer_load_dwordx4 v[32:35], v6, s[40:43], s49 offen offset:0 sc1\n"
    "buffer_load_dwordx4 v[72:75], v6, s[40:43], s49 offen offset:1024 sc1\n"
    "buffer_load_dwordx4 v[76:79], v6, s[40:43], s49 offen offset:2048 sc1\n"
    "buffer_load_dwordx4 v[80:83], v6, s[40:43], s49 offen offset:3072 sc1\n"
    "buffer_load_dwordx4 v[84:87], v6, s[40:43], s50 offen offset:0 sc1\n"
    "buffer_load_dwordx4 v[88:91], v6, s[40:43], s50 offen offset:1024 sc1\n"
    "buffer_load_dwordx4 v[92:95], v6, s[40:43], s50 offen offset:2048 sc1\n"
    "buffer_load_dwordx4 v[96:99], v6, s[40:43], s50 offen offset:3072 sc1\n"
    "buffer_load_dwordx4 v[100:103], v6, s[40:43], s51 offen offset:0 sc1\n"
    "buffer_load_dwordx4 v[104:107], v6, s[40:43], s51 offen offset:1024 sc1\n"
    "buffer_load_dwordx4 v[108:111], v6, s[40:43], s51 offen offset:2048 sc1\n"
    "buffer_load_dwordx4 v[112:115], v6, s[40:43], s51 offen offset:3072 sc1\n"
    "s_sub_u32 s12, s97, 7340032\n"
    "s_lshr_b32 s13, s12, 13\n"
    "s_and_b32 s13, s13, 0x3f0\n"
    "s_or_b32 s13, s13, s3\n"
    "s_lshl_b32 s12, s13, 13\n"
    "s_add_u32 s12, s12, 15728640\n"
    "s_lshl_b32 s13, s13, 7\n"
    "s_add_u32 s13, s13, 131072\n"
    "s_mul_i32 s14, s6, 3\n"
    "s_sub_u32 s14, s3, s14\n"
    "s_cmp_lt_u32 s14, 3\n"
    "s_cbranch_scc1 L_noprio\n"
    "s_lshl_b32 s56, s6, 11\n"
    "s_add_u32 s12, s12, s56\n"
    "s_lshl_b32 s56, s6, 3\n"
    "s_add_u32 s13, s13, s56\n"
    "s_setprio 1\n"
    "L_noprio:\n"
    "s_waitcnt lgkmcnt(0)\n"
    "s_barrier\n"
    "ds_read_b128 v[36:39], v5 offset:0\n"
    "ds_read_b128 v[48:51], v5 offset:1024\n"
    "ds_read_b128 v[52:55], v5 offset:2048\n"
    "s_waitcnt vmcnt(23) lgkmcnt(2)\n"
    "v_mfma_f32_32x32x16_bf16 v[56:71], v[148:151], v[36:39], 0\n"
    "ds_read_b128 v[36:39], v5 offset:3072\n"
    "s_waitcnt vmcnt(22) lgkmcnt(2)\n"
    "v_mfma_f32_32x32x16_bf16 v[56:71], v[144:147], v[48:51], v[56:71]\n"
    "ds_read_b128 v[48:51], v5 offset:4096\n"
    "s_waitcnt vmcnt(21) lgkmcnt(2)\n"
    "v_mfma_f32_32x32x16_bf16 v[56:71], v[136:139], v[52:55], v[56:71]\n"
    "ds_read_b128 v[52:55], v5 offset:5120\n"
    "s_waitcnt vmcnt(20) lgkmcnt(2)\n"
    "v_mfma_f32_32x32x16_bf16 v[56:71], v[132:135], v[36:39], v[56:71]\n"
    "ds_read_b128 v[36:39], v5 offset:6144\n"
    "s_waitcnt vmcnt(19) lgkmcnt(2)\n"
    "v_mfma_f32_32x32x16_bf16 v[56:71], v[128:131], v[48:51], v[56:71]\n"
    "ds_read_b128 v[48:51], v5 offset:7168\n"
    "s_waitcnt vmcnt(18) lgkmcnt(2)\n"
    "v_mfma_f32_32x32x16_bf16 v[56:71], v[124:127], v[52:55], v[56:71]\n"
    "ds_read_b128 v[52:55], v5 offset:8192\n"
    "s_waitcnt vmcnt(17) lgkmcnt(2)\n"
    "v_mfma_f32_32x32x16_bf16 v[56:71], v[8:11], v[36:39], v[56:71]\n"
    "ds_read_b128 v[36:39], v5 offset:9216\n"
    "s_waitcnt vmcnt(16) lgkmcnt(2)\n"
    "v_mfma_f32_32x32x16_bf16 v[56:71], v[12:15], v[48:51], v[56:71]\n"
    "ds_read_b128 v[48:51], v5 offset:10240\n"
    "s_waitcnt vmcnt(15) lgkmcnt(2)\n"
    "v_mfma_f32_32x32x16_bf16 v[56:71], v[16:19], v[52:55], v[56:71]\n"
    "ds_read_b128 v[52:55], v5 offset:11264\n"
    "s_waitcnt vmcnt(14) lgkmcnt(2)\n"
    "v_mfma_f32_32x32x16_bf16 v[56:71], v[20:23], v[36:39], v[56:71]\n"
    "ds_read_b128 v[36:39], v5 offset:12288\n"
    "s_waitcnt vmcnt(13) lgkmcnt(2)\n"
    "v_mfma_f32_32x32x16_bf16 v[56:71], v[24:27], v[48:51], v[56:71]\n"
    "ds_read_b128 v[48:51], v5 offset:13312\n"
    "s_waitcnt vmcnt(12) lgkmcnt(2)\n"
    "v_mfma_f32_32x32x16_bf16 v[56:71], v[28:31], v[52:55], v[56:71]\n"
    "ds_read_b128 v[52:55], v5 offset:14336\n"
    "s_waitcnt vmcnt(11) lgkmcnt(2)\n"
    "v_mfma_f32_32x32x16_bf16 v[56:71], v[32:35], v[36:39], v[56:71]\n"
    "ds_read_b128 v[36:39], v5 offset:15360\n"
    "s_waitcnt vmcnt(10) lgkmcnt(2)\n"
    "v_mfma_f32_32x32x16_bf16 v[56:71], v[72:75], v[48:51], v[56:71]\n"
    "ds_read_b128 v[48:51], v5 offset:16384\n"
    "s_waitcnt vmcnt(9) lgkmcnt(2)\n"
    "v_mfma_f32_32x32x16_bf16 v[56:71], v[76:79], v[52:55], v[56:71]\n"
    "ds_read_b128 v[52:55], v5 offset:17408\n"
    "s_waitcnt vmcnt(8) lgkmcnt(2)\n"
    "v_mfma_f32_32x32x16_bf16 v[56:71], v[80:83], v[36:39], v[56:71]\n"
    "ds_read_b128 v[36:39], v5 offset:18432\n"
    "s_waitcnt vmcnt(7) lgkmcnt(2)\n"
    "v_mfma_f32_32x32x16_bf16 v[56:71], v[84:87], v[48:51], v[56:71]\n"
    "ds_read_b128 v[48:51], v5 offset:19456\n"
    "s_waitcnt vmcnt(6) lgkmcnt(2)\n"
    "v_mfma_f32_32x32x16_bf16 v[56:71], v[88:91], v[52:55], v[56:71]\n"
    "ds_read_b128 v[52:55], v5 offset:20480\n"
    "s_waitcnt vmcnt(5) lgkmcnt(2)\n"
    "v_mfma_f32_32x32x16_bf16 v[56:71], v[92:95], v[36:39], v[56:71]\n"
    "ds_read_b128 v[36:39], v5 offset:21504\n"
    "s_waitcnt vmcnt(4) lgkmcnt(2)\n"
    "v_mfma_f32_32x32x16_bf16 v[56:71], v[96:99], v[48:51], v[56:71]\n"
    "ds_read_b128 v[48:51], v5 offset:22528\n"
    "s_waitcnt vmcnt(3) lgkmcnt(2)\n"
    "v_mfma_f32_32x32x16_bf16 v[56:71], v[100:103], v[52:55], v[56:71]\n"
    "ds_read_b128 v[52:55], v5 offset:23552\n"
    "s_waitcnt vmcnt(2) lgkmcnt(2)\n"
    "v_mfma_f32_32x32x16_bf16 v[56:71], v[104:107], v[36:39], v[56:71]\n"
    "s_waitcnt vmcnt(1) lgkmcnt(1)\n"
    "v_mfma_f32_32x32x16_bf16 v[56:71], v[108:111], v[48:51], v[56:71]\n"
    "s_waitcnt vmcnt(0) lgkmcnt(0)\n"
    "v_mfma_f32_32x32x16_bf16 v[56:71], v[112:115], v[52:55], v[56:71]\n"
    "s_cmp_lt_u32 s14, 3\n"
    "s_cbranch_scc1 L_owner\n"
    "s_nop 9\n"
    "v_cvt_pk_bf16_f32 v8, v56, v57\n"
    "v_cvt_pk_bf16_f32 v9, v58, v59\n"
    "v_cvt_pk_bf16_f32 v10, v60, v61\n"
    "v_cvt_pk_bf16_f32 v11, v62, v63\n"
    "buffer_store_dwordx4 v[8:11], v0, s[40:43], s12 offen sc1\n"
    "v_cvt_pk_bf16_f32 v12, v64, v65\n"
    "v_cvt_pk_bf16_f32 v13, v66, v67\n"
    "v_cvt_pk_bf16_f32 v14, v68, v69\n"
    "v_cvt_pk_bf16_f32 v15, v70, v71\n"
    "buffer_store_dwordx4 v[12:15], v0, s[40:43], s12 offen offset:1024 sc1\n"
    "s_waitcnt vmcnt(0)\n"
    "s_mov_b64 exec, 1\n"
    "buffer_store_dwordx2 v[2:3], off, s[40:43], s13 sc1\n"
    "s_mov_b64 exec, -1\n"
    "s_barrier\n"
    "s_or_b32 s56, s5, s4\n"
    "s_sub_u32 s57, s3, 11\n"
    "s_or_b32 s56, s56, s57\n"
    "s_cbranch_scc1 L_end\n"
    "v_lshrrev_b32 v5, 3, v1\n"
    "v_lshlrev_b32 v5, 11, v5\n"
    "v_and_b32 v6, 7, v1\n"
    "v_lshl_add_u32 v5, v6, 3, v5\n"
    "s_mov_b32 s56, 66560\n"
    "buffer_load_dwordx2 v[16:17], v5, s[40:43], s56 offen sc1\n"
    "s_movk_i32 s46, 32767\n"
    "L_ep_29:\n"
    "s_sub_u32 s46, s46, 1\n"
    "s_waitcnt vmcnt(0)\n"
    "v_cmp_ne_u64 vcc, s[44:45], v[16:17]\n"
    "s_cbranch_vccz L_ep_ok_30\n"
    "buffer_load_dwordx2 v[16:17], v5, s[40:43], s56 offen sc1\n"
    "s_cbranch_scc0 L_ep_29\n"
    "L_ep_ok_30:\n"
    "s_mov_b32 s56, 82944\n"
    "s_waitcnt vmcnt(0)\n"
    "buffer_load_dwordx2 v[16:17], v5, s[40:43], s56 offen sc1\n"
    "s_movk_i32 s46, 32767\n"
    "L_ep_31:\n"
    "s_sub_u32 s46, s46, 1\n"
    "s_waitcnt vmcnt(0)\n"
    "v_cmp_ne_u64 vcc, s[44:45], v[16:17]\n"
    "s_cbranch_vccz L_ep_ok_32\n"
    "buffer_load_dwordx2 v[16:17], v5, s[40:43], s56 offen sc1\n"
    "s_cbranch_scc0 L_ep_31\n"
    "L_ep_ok_32:\n"
    "s_mov_b32 s56, 99328\n"
    "s_waitcnt vmcnt(0)\n"
    "buffer_load_dwordx2 v[16:17], v5, s[40:43], s56 offen sc1\n"
    "s_movk_i32 s46, 32767\n"
    "L_ep_33:\n"
    "s_sub_u32 s46, s46, 1\n"
    "s_waitcnt vmcnt(0)\n"
    "v_cmp_ne_u64 vcc, s[44:45], v[16:17]\n"
    "s_cbranch_vccz L_ep_ok_34\n"
    "buffer_load_dwordx2 v[16:17], v5, s[40:43], s56 offen sc1\n"
    "s_cbranch_scc0 L_ep_33\n"
    "L_ep_ok_34:\n"
    "s_mov_b32 s56, 115712\n"
    "s_waitcnt vmcnt(0)\n"
    "buffer_load_dwordx2 v[16:17], v5, s[40:43], s56 offen sc1\n"
    "s_movk_i32 s46, 32767\n"
    "L_ep_35:\n"
    "s_sub_u32 s46, s46, 1\n"
    "s_waitcnt vmcnt(0)\n"
    "v_cmp_ne_u64 vcc, s[44:45], v[16:17]\n"
    "s_cbranch_vccz L_ep_ok_36\n"
    "buffer_load_dwordx2 v[16:17], v5, s[40:43], s56 offen sc1\n"
    "s_cbranch_scc0 L_ep_35\n"
    "L_ep_ok_36:\n"
    "s_mov_b64 exec, 1\n"
    "buffer_store_dword v2, off, s[40:43], 0 sc1\n"
    "s_mov_b64 exec, -1\n"
    "s_branch L_end\n"
    "L_owner:\n"
    "ds_read_b128 v[16:19], v120 offset:4608\n"
    "ds_read_b128 v[20:23], v120 offset:4640\n"
    "ds_read_b128 v[24:27], v120 offset:4672\n"
    "ds_read_b128 v[28:31], v120 offset:4704\n"
    "v_min_u32 v5, 2, v1\n"
    "s_add_u32 s56, s6, 1\n"
    "v_add_u32 v5, s56, v5\n"
    "v_and_b32 v5, 3, v5\n"
    "v_lshlrev_b32 v5, 3, v5\n"
    "s_add_u32 s56, s6, 1\n"
    "s_and_b32 s56, s56, 3\n"
    "s_lshl_b32 s56, s56, 11\n"
    "s_add_u32 s16, s12, s56\n"
    "s_add_u32 s56, s6, 2\n"
    "s_and_b32 s56, s56, 3\n"
    "s_lshl_b32 s56, s56, 11\n"
    "s_add_u32 s17, s12, s56\n"
    "s_add_u32 s56, s6, 3\n"
    "s_and_b32 s56, s56, 3\n"
    "s_lshl_b32 s56, s56, 11\n"
    "s_add_u32 s18, s12, s56\n"
    "v_and_b32 v6, 31, v1\n"
    "v_mul_u32_u24 v6, 768, v6\n"
    "v_lshrrev_b32 v7, 5, v1\n"
    "v_lshl_add_u32 v6, v7, 4, v6\n"
    "v_lshlrev_b32 v100, 16, v40\n"
    "v_and_b32 v101, 0xffff0000, v40\n"
    "s_waitcnt lgkmcnt(3)\n"
    "v_pk_fma_f32 v[56:57], v[56:57], v[16:17], v[100:101]\n"
    "v_lshlrev_b32 v100, 16, v41\n"
    "v_and_b32 v101, 0xffff0000, v41\n"
    "v_pk_fma_f32 v[58:59], v[58:59], v[18:19], v[100:101]\n"
    "v_lshlrev_b32 v100, 16, v42\n"
    "v_and_b32 v101, 0xffff0000, v42\n"
    "s_waitcnt lgkmcnt(2)\n"
    "v_pk_fma_f32 v[60:61], v[60:61], v[20:21], v[100:101]\n"
    "v_lshlrev_b32 v100, 16, v43\n"
    "v_and_b32 v101, 0xffff0000, v43\n"
    "v_pk_fma_f32 v[62:63], v[62:63], v[22:23], v[100:101]\n"
    "v_lshlrev_b32 v100, 16, v44\n"
    "v_and_b32 v101, 0xffff0000, v44\n"
    "s_waitcnt lgkmcnt(1)\n"
    "v_pk_fma_f32 v[64:65], v[64:65], v[24:25], v[100:101]\n"
    "v_lshlrev_b32 v100, 16, v45\n"
    "v_and_b32 v101, 0xffff0000, v45\n"
    "v_pk_fma_f32 v[66:67], v[66:67], v[26:27], v[100:101]\n"
    "v_lshlrev_b32 v100, 16, v46\n"
    "v_and_b32 v101, 0xffff0000, v46\n"
    "s_waitcnt lgkmcnt(0)\n"
    "v_pk_fma_f32 v[68:69], v[68:69], v[28:29], v[100:101]\n"
    "v_lshlrev_b32 v100, 16, v47\n"
    "v_and_b32 v101, 0xffff0000, v47\n"
    "v_pk_fma_f32 v[70:71], v[70:71], v[30:31], v[100:101]\n"
    "s_barrier\n"
    "buffer_load_dwordx2 v[96:97], v5, s[40:43], s13 offen sc1\n"
    "s_movk_i32 s46, 32767\n"
    "L_fm_37:\n"
    "s_sub_u32 s46, s46, 1\n"
    "s_waitcnt vmcnt(0)\n"
    "v_cmp_ne_u64 vcc, s[44:45], v[96:97]\n"
    "s_cbranch_vccz L_fm_ok_38\n"
    "buffer_load_dwordx2 v[96:97], v5, s[40:43], s13 offen sc1\n"
    "s_cbranch_scc0 L_fm_37\n"
    "L_fm_ok_38:\n"
    "buffer_load_dwordx4 v[72:75], v0, s[40:43], s16 offen offset:0 sc1\n"
    "buffer_load_dwordx4 v[76:79], v0, s[40:43], s17 offen offset:0 sc1\n"
    "buffer_load_dwordx4 v[80:83], v0, s[40:43], s18 offen offset:0 sc1\n"
    "buffer_load_dwordx4 v[84:87], v0, s[40:43], s16 offen offset:1024 sc1\n"
    "buffer_load_dwordx4 v[88:91], v0, s[40:43], s17 offen offset:1024 sc1\n"
    "buffer_load_dwordx4 v[92:95], v0, s[40:43], s18 offen offset:1024 sc1\n"
    "s_waitcnt vmcnt(5)\n"
    "v_lshlrev_b32 v100, 16, v72\n"
    "v_and_b32 v101, 0xffff0000, v72\n"
    "v_pk_fma_f32 v[56:57], v[100:101], v[16:17], v[56:57]\n"
    "s_waitcnt vmcnt(4)\n"
    "v_lshlrev_b32 v102, 16, v76\n"
    "v_and_b32 v103, 0xffff0000, v76\n"
    "v_pk_fma_f32 v[56:57], v[102:103], v[16:17], v[56:57]\n"
    "s_waitcnt vmcnt(3)\n"
    "v_lshlrev_b32 v100, 16, v80\n"
    "v_and_b32 v101, 0xffff0000, v80\n"
    "v_pk_fma_f32 v[56:57], v[100:101], v[16:17], v[56:57]\n"
    "v_cvt_pk_bf16_f32 v8, v56, v57\n"
    "v_lshlrev_b32 v100, 16, v73\n"
    "v_and_b32 v101, 0xffff0000, v73\n"
    "v_pk_fma_f32 v[58:59], v[100:101], v[18:19], v[58:59]\n"
    "v_lshlrev_b32 v102, 16, v77\n"
    "v_and_b32 v103, 0xffff0000, v77\n"
    "v_pk_fma_f32 v[58:59], v[102:103], v[18:19], v[58:59]\n"
    "v_lshlrev_b32 v100, 16, v81\n"
    "v_and_b32 v101, 0xffff0000, v81\n"
    "v_pk_fma_f32 v[58:59], v[100:101], v[18:19], v[58:59]\n"
    "v_cvt_pk_bf16_f32 v9, v58, v59\n"
    "v_lshlrev_b32 v100, 16, v74\n"
    "v_and_b32 v101, 0xffff0000, v74\n"
    "v_pk_fma_f32 v[60:61], v[100:101], v[20:21], v[60:61]\n"
    "v_lshlrev_b32 v102, 16, v78\n"
    "v_and_b32 v103, 0xffff0000, v78\n"
    "v_pk_fma_f32 v[60:61], v[102:103], v[20:21], v[60:61]\n"
    "v_lshlrev_b32 v100, 16, v82\n"
    "v_and_b32 v101, 0xffff0000, v82\n"
    "v_pk_fma_f32 v[60:61], v[100:101], v[20:21], v[60:61]\n"
    "v_cvt_pk_bf16_f32 v10, v60, v61\n"
    "v_lshlrev_b32 v100, 16, v75\n"
    "v_and_b32 v101, 0xffff0000, v75\n"
    "v_pk_fma_f32 v[62:63], v[100:101], v[22:23], v[62:63]\n"
    "v_lshlrev_b32 v102, 16, v79\n"
    "v_and_b32 v103, 0xffff0000, v79\n"
    "v_pk_fma_f32 v[62:63], v[102:103], v[22:23], v[62:63]\n"
    "v_lshlrev_b32 v100, 16, v83\n"
    "v_and_b32 v101, 0xffff0000, v83\n"
    "v_pk_fma_f32 v[62:63], v[100:101], v[22:23], v[62:63]\n"
    "v_cvt_pk_bf16_f32 v11, v62, v63\n"
    "v_permlane32_swap_b32 v8, v10\n"
    "s_nop 0\n"
    "v_permlane32_swap_b32 v9, v11\n"
    "buffer_store_dwordx4 v[8:11], v6, s[36:39], s63 offen offset:0\n"
    "s_waitcnt vmcnt(3)\n"
    "v_lshlrev_b32 v100, 16, v84\n"
    "v_and_b32 v101, 0xffff0000, v84\n"
    "v_pk_fma_f32 v[64:65], v[100:101], v[24:25], v[64:65]\n"
    "s_waitcnt vmcnt(2)\n"
    "v_lshlrev_b32 v102, 16, v88\n"
    "v_and_b32 v103, 0xffff0000, v88\n"
    "v_pk_fma_f32 v[64:65], v[102:103], v[24:25], v[64:65]\n"
    "s_waitcnt vmcnt(1)\n"
    "v_lshlrev_b32 v100, 16, v92\n"
    "v_and_b32 v101, 0xffff0000, v92\n"
    "v_pk_fma_f32 v[64:65], v[100:101], v[24:25], v[64:65]\n"
    "v_cvt_pk_bf16_f32 v12, v64, v65\n"
    "v_lshlrev_b32 v100, 16, v85\n"
    "v_and_b32 v101, 0xffff0000, v85\n"
    "v_pk_fma_f32 v[66:67], v[100:101], v[26:27], v[66:67]\n"
    "v_lshlrev_b32 v102, 16, v89\n"
    "v_and_b32 v103, 0xffff0000, v89\n"
    "v_pk_fma_f32 v[66:67], v[102:103], v[26:27], v[66:67]\n"
    "v_lshlrev_b32 v100, 16, v93\n"
    "v_and_b32 v101, 0xffff0000, v93\n"
    "v_pk_fma_f32 v[66:67], v[100:101], v[26:27], v[66:67]\n"
    "v_cvt_pk_bf16_f32 v13, v66, v67\n"
    "v_lshlrev_b32 v100, 16, v86\n"
    "v_and_b32 v101, 0xffff0000, v86\n"
    "v_pk_fma_f32 v[68:69], v[100:101], v[28:29], v[68:69]\n"
    "v_lshlrev_b32 v102, 16, v90\n"
    "v_and_b32 v103, 0xffff0000, v90\n"
    "v_pk_fma_f32 v[68:69], v[102:103], v[28:29], v[68:69]\n"
    "v_lshlrev_b32 v100, 16, v94\n"
    "v_and_b32 v101, 0xffff0000, v94\n"
    "v_pk_fma_f32 v[68:69], v[100:101], v[28:29], v[68:69]\n"
    "v_cvt_pk_bf16_f32 v14, v68, v69\n"
    "v_lshlrev_b32 v100, 16, v87\n"
    "v_and_b32 v101, 0xffff0000, v87\n"
    "v_pk_fma_f32 v[70:71], v[100:101], v[30:31], v[70:71]\n"
    "v_lshlrev_b32 v102, 16, v91\n"
    "v_and_b32 v103, 0xffff0000, v91\n"
    "v_pk_fma_f32 v[70:71], v[102:103], v[30:31], v[70:71]\n"
    "v_lshlrev_b32 v100, 16, v95\n"
    "v_and_b32 v101, 0xffff0000, v95\n"
    "v_pk_fma_f32 v[70:71], v[100:101], v[30:31], v[70:71]\n"
    "v_cvt_pk_bf16_f32 v15, v70, v71\n"
    "v_permlane32_swap_b32 v12, v14\n"
    "s_nop 0\n"
    "v_permlane32_swap_b32 v13, v15\n"
    "buffer_store_dwordx4 v[12:15], v6, s[36:39], s63 offen offset:32\n"
    "L_end:\n"
    "s_endpgm\n"
    : "+{v0}"(tid), "+{s[0:1]}"(ka), "+{s2}"(bid)
    :
    : "v1", "v2", "v3", "v4", "v5", "v6", "v7", "v8", "v9", "v10", "v11", "v12", "v13", "v14", "v15", "v16", "v17", "v18", "v19", "v20", "v21", "v22", "v23", "v24", "v25", "v26", "v27", "v28", "v29", "v30", "v31", "v32", "v33", "v34", "v35", "v36", "v37", "v38", "v39", "v40", "v41", "v42", "v43", "v44", "v45", "v46", "v47", "v48", "v49", "v50", "v51", "v52", "v53", "v54", "v55", "v56", "v57", "v58", "v59", "v60", "v61", "v62", "v63", "v64", "v65", "v66", "v67", "v68", "v69", "v70", "v71", "v72", "v73", "v74", "v75", "v76", "v77", "v78", "v79", "v80", "v81", "v82", "v83", "v84", "v85", "v86", "v87", "v88", "v89", "v90", "v91", "v92", "v93", "v94", "v95", "v96", "v97", "v98", "v99", "v100", "v101", "v102", "v103", "v104", "v105", "v106", "v107", "v108", "v109", "v110", "v111", "v112", "v113", "v114", "v115", "v116", "v117", "v118", "v119", "v120", "v121", "v122", "v123", "v124", "v125", "v126", "v127", "v128", "v129", "v130", "v131", "v132", "v133", "v134", "v135", "v136", "v137", "v138", "v139", "v140", "v141", "v142", "v143", "v144", "v145", "v146", "v147", "v148", "v149", "v150", "v151", "v152", "v153", "v154", "v155", "v156", "v157", "v158", "v159", "v160", "v161", "v162", "v163", "v164", "v165", "v166", "v167", "s3", "s4", "s5", "s6", "s7", "s8", "s9", "s10", "s11", "s12", "s13", "s14", "s15", "s16", "s17", "s18", "s19", "s20", "s21", "s22", "s23", "s24", "s25", "s26", "s27", "s28", "s29", "s30", "s31", "s33", "s34", "s35", "s36", "s37", "s38", "s39", "s40", "s41", "s42", "s43", "s44", "s45", "s46", "s47", "s48", "s49", "s50", "s51", "s52", "s53", "s54", "s55", "s56", "s57", "s58", "s59", "s60", "s61", "s62", "s63", "s64", "s65", "s66", "s67", "s68", "s69", "s70", "s71", "s72", "s73", "s74", "s75", "s76", "s77", "s78", "s79", "s80", "s81", "s82", "s83", "s84", "s85", "s86", "s87", "s88", "s89", "s90", "s91", "s92", "s93", "s94", "s95", "s96", "s97", "s98", "s99", "vcc", "scc", "memory");
}

extern "C" void kernel_launch(void* const* d_in, const int* in_sizes, int n_in, void* d_out, int out_size, void* d_ws,
                              size_t ws_size, hipStream_t stream) {
  constexpr unsigned F = 0x00020000u;
  dit_block<<<256, 768, 0, stream>>>(d_in[0], 1572864u, F, d_in[1], 1536u, F, d_in[2], 884736u, F, d_in[3], 294912u, F,
                                     d_in[4], 1769472u, F, d_in[5], 1179648u, F, d_in[6], 1179648u, F,
                                     d_out, 1572864u, F, d_ws, WS_BYTES, F);
}
```
